```python
import torch

def _softmax(x, axis=-1):
    return torch.softmax(x, dim=axis)

def _gelu(x):
    return torch.nn.functional.gelu(x, approximate='tanh')

def _silu(x):
    return torch.nn.functional.silu(x)

def _sigmoid(x):
    return torch.sigmoid(x)

def _layernorm(x, axis=-1, eps=1e-05):
    m = x.mean(dim=axis, keepdim=True)
    v = x.var(dim=axis, keepdim=True, unbiased=False)
    return (x - m) / torch.sqrt(v + eps)

def _scatter_add(tgt, idx, src, axis=0):
    return tgt.clone().index_add_(axis, idx, src)

def _segment_sum(src, seg, n):
    out = torch.zeros((n,) + src.shape[1:], dtype=src.dtype)
    return out.index_add_(0, seg, src)

def _pad(x, pw):
    flat = tuple((v for pair in reversed(pw) for v in pair))
    return torch.nn.functional.pad(x, flat)
kernel_name = 'adaLN_cond__grouped_kv_mqa__geglu_ffn__prompt'

def setup_inputs():

    _w = lambda a: (lambda t: t.to(torch.bfloat16) if t.is_floating_point() else t)(torch.as_tensor(a))

    def _A_setup():
        B, S, D, H = (1, 2048, 512, 32)
        Dh = D // H
        return {'x': _w((lambda *s: torch.randn(*s, dtype=torch.bfloat16) * 0.05)(B, S, D)), 'wq': _w((lambda *s: torch.randn(*s, dtype=torch.bfloat16) * 0.1)(D, H * Dh)), 'wk': _w((lambda *s: torch.randn(*s, dtype=torch.bfloat16) * 0.1)(D, Dh)), 'wv': _w((lambda *s: torch.randn(*s, dtype=torch.bfloat16) * 0.05)(D, Dh)), 'wo': _w((lambda *s: torch.randn(*s, dtype=torch.bfloat16) * 0.05)(H * Dh, D))}
    _A_in = _A_setup()

    def _B_setup():
        B, S, D, F = (1, 2048, 512, 1792)
        return {'x': _w((lambda *s: torch.randn(*s, dtype=torch.bfloat16) * 0.05)(B, S, D)), 'w_gate': _w((lambda *s: torch.randn(*s, dtype=torch.bfloat16) * 0.05)(D, F)), 'w_up': _w((lambda *s: torch.randn(*s, dtype=torch.bfloat16) * 0.05)(D, F)), 'w_down': _w((lambda *s: torch.randn(*s, dtype=torch.bfloat16) * 0.05)(F, D))}
    _B_in = _B_setup()
    return {'A_x': _A_in['x'], 'A_wq': _A_in['wq'], 'A_wk': _A_in['wk'], 'A_wv': _A_in['wv'], 'A_wo': _A_in['wo'], 'B_x': _B_in['x'], 'B_w_gate': _B_in['w_gate'], 'B_w_up': _B_in['w_up'], 'B_w_down': _B_in['w_down'], 'c': _w((lambda *s: torch.randn(*s, dtype=torch.bfloat16) * 0.1)(1, 512) * 8.0), 'w_mod': _w((lambda *s: torch.randn(*s, dtype=torch.bfloat16) * 0.1)(512, 6 * 512) * 512 ** (-0.5) * 8.0)}

def reference(A_x, A_wq, A_wk, A_wv, A_wo, B_x, B_w_gate, B_w_up, B_w_down, c, w_mod):

    def _A_ref(x, wq, wk, wv, wo):
        B, S, D = x.shape
        H = 32
        Dh = D // H
        q = (lambda x, *d: x.permute(*d))((x @ wq).reshape(B, S, H, Dh), 0, 2, 1, 3)
        k = x @ wk
        v = x @ wv
        scores = torch.einsum('bhqd,bkd->bhqk', q, k) * Dh ** (-0.5)
        p = _softmax(scores, axis=-1)
        o = torch.einsum('bhqk,bkd->bhqd', p, v)
        return (lambda x, *d: x.permute(*d))(o, 0, 2, 1, 3).reshape(B, S, H * Dh) @ wo

    def _B_ref(x, w_gate, w_up, w_down):
        return _gelu(x @ w_gate) * (x @ w_up) @ w_down
    x = A_x
    mod = (_silu(c) @ w_mod).reshape(c.shape[0], 6, 1, -1)
    sa, ca, ga, sb, cb, gb = (mod[:, 0].clone(), mod[:, 1].clone(), mod[:, 2].clone(), mod[:, 3].clone(), mod[:, 4].clone(), mod[:, 5].clone())
    h = _layernorm(x, axis=-1) * (1 + ca) + sa
    x = x + ga * _A_ref(h, A_wq, A_wk, A_wv, A_wo)
    h = _layernorm(x, axis=-1) * (1 + cb) + sb
    x = x + gb * _B_ref(h, B_w_gate, B_w_up, B_w_down)
    return x
```

```cpp
#include <hip/hip_runtime.h>
#include <hip/hip_bf16.h>
#include <stdint.h>

typedef __attribute__((ext_vector_type(8))) short bf16x8;
typedef __attribute__((ext_vector_type(4))) short s16x4;
typedef __attribute__((ext_vector_type(2))) float f32x2;
typedef __attribute__((ext_vector_type(4))) float f32x4;
typedef __attribute__((ext_vector_type(16))) float f32x16;
typedef __attribute__((ext_vector_type(2))) unsigned u32x2;
typedef __attribute__((ext_vector_type(3))) unsigned u32x3;
typedef __attribute__((ext_vector_type(4))) unsigned u32x4;
typedef __attribute__((ext_vector_type(2))) __bf16 bf16x2_t;
typedef short v4i16_t __attribute__((ext_vector_type(4)));
#define LAS __attribute__((address_space(3)))
#define DI __device__ __forceinline__

constexpr int SEQ = 2048, DM = 512, FF = 1792;

constexpr size_t WS_MOD  = 0;
constexpr size_t WS_KMAX = 16384;
constexpr size_t WS_WQKV = 32768;
constexpr size_t WS_WO   = WS_WQKV + 34 * 16 * 1024;
constexpr size_t WS_WGU  = WS_WO + 512 * 1024;
constexpr size_t WS_WD   = WS_WGU + 32 * 16 * 7 * 1024;
constexpr size_t WS_Q    = WS_WD + 8 * 112 * 2 * 1024;
constexpr size_t WS_KT   = WS_Q + SEQ * DM * 2;
constexpr size_t WS_VT   = WS_KT + 65536;
constexpr size_t WS_X2   = WS_VT + 65536;
constexpr size_t WS_H2   = WS_X2 + SEQ * DM * 2;
constexpr size_t WS_ACT  = WS_H2 + SEQ * DM * 2;

DI unsigned cvtpk(float lo, float hi) { f32x2 v = {lo, hi}; bf16x2_t b = __builtin_convertvector(v, bf16x2_t); return __builtin_bit_cast(unsigned, b); }
DI float bflo(unsigned u) { return __uint_as_float(u << 16); }
DI float bfhi(unsigned u) { return __uint_as_float(u & 0xffff0000u); }
DI float rbf(float x) { return bflo(cvtpk(x, x)); }
DI s16x4 trread(const LAS char* p) { return __builtin_bit_cast(s16x4, __builtin_amdgcn_ds_read_tr16_b64_v4i16((LAS v4i16_t*)p)); }
DI f32x4 mfma16(u32x4 a, u32x4 b, f32x4 c) { return __builtin_amdgcn_mfma_f32_16x16x32_bf16(__builtin_bit_cast(bf16x8, a), __builtin_bit_cast(bf16x8, b), c, 0, 0, 0); }
DI f32x16 mfma32(u32x4 a, u32x4 b, f32x16 c) { return __builtin_amdgcn_mfma_f32_32x32x16_bf16(__builtin_bit_cast(bf16x8, a), __builtin_bit_cast(bf16x8, b), c, 0, 0, 0); }
template <int CTRL> DI float dpp_add(float v) { return v + __uint_as_float(__builtin_amdgcn_update_dpp(0, __float_as_uint(v), CTRL, 0xf, 0xf, true)); }
template <int CTRL> DI float dpp_max(float v) { return fmaxf(v, __uint_as_float(__builtin_amdgcn_update_dpp(0, __float_as_uint(v), CTRL, 0xf, 0xf, true))); }
DI float row16_sum(float v) { v = dpp_add<0x128>(v); v = dpp_add<0x124>(v); v = dpp_add<0x122>(v); v = dpp_add<0x121>(v); return v; }
DI float row16_max(float v) { v = dpp_max<0x128>(v); v = dpp_max<0x124>(v); v = dpp_max<0x122>(v); v = dpp_max<0x121>(v); return v; }
DI float swap32_sum(float v) { auto r = __builtin_amdgcn_permlane32_swap(__float_as_uint(v), __float_as_uint(v), false, false); return __uint_as_float(r[0]) + __uint_as_float(r[1]); }
DI float swap16_sum(float v) { auto r = __builtin_amdgcn_permlane16_swap(__float_as_uint(v), __float_as_uint(v), false, false); return __uint_as_float(r[0]) + __uint_as_float(r[1]); }
DI float swap32_max(float v) { auto r = __builtin_amdgcn_permlane32_swap(__float_as_uint(v), __float_as_uint(v), false, false); return fmaxf(__uint_as_float(r[0]), __uint_as_float(r[1])); }
DI float swap16_max(float v) { auto r = __builtin_amdgcn_permlane16_swap(__float_as_uint(v), __float_as_uint(v), false, false); return fmaxf(__uint_as_float(r[0]), __uint_as_float(r[1])); }
#define LDS_BAR() do { asm volatile("s_waitcnt lgkmcnt(0)" ::: "memory"); __builtin_amdgcn_s_barrier(); asm volatile("" ::: "memory"); } while (0)
#define SBAR() __builtin_amdgcn_sched_barrier(0)

constexpr int K1_MOD = 96, K1_WQ = 64, K1_WKV = 4, K1_WO = 64, K1_WGU = 128, K1_WD = 64;
constexpr int K1_GRID = K1_MOD + K1_WQ + K1_WKV + K1_WO + K1_WGU + K1_WD;

DI int wd_row(int sigma, int j) {
    const int ft = sigma >= 7 ? 1 : 0, s = sigma - 7 * ft, i = j >> 1, e = j & 1;
    int g, b;
    if (s < 4) { g = s; b = i; } else if (i < 3) { g = s - 4; b = 4 + i; } else { g = 3; b = s; }
    return 56 * ft + 8 * b + 2 * g + e;
}

__global__ __launch_bounds__(256) void k1_prep(const uint16_t* __restrict__ c, const uint16_t* __restrict__ wmod,
                                               const uint16_t* __restrict__ wq, const uint16_t* __restrict__ wk, const uint16_t* __restrict__ wv,
                                               const uint16_t* __restrict__ wo, const uint16_t* __restrict__ wg, const uint16_t* __restrict__ wu,
                                               const uint16_t* __restrict__ wd, char* __restrict__ ws) {
    __shared__ __attribute__((aligned(16))) char smem[32768];
    const LAS char* lds = (const LAS char*)smem;
    const int tid = threadIdx.x, lane = tid & 63, wid = tid >> 6;
    const int q = (lane & 15) >> 2, p = lane & 3, grp = lane >> 4;
    int b = blockIdx.x;
    if (b < K1_MOD) {
        const int cq = tid & 3, rg = tid >> 2, c0 = b * 32;
        u32x4 w[8]; float s[8];
#pragma unroll
        for (int i = 0; i < 8; ++i) w[i] = *(const u32x4*)(wmod + (size_t)(rg + 64 * i) * 3072 + c0 + cq * 8);
#pragma unroll
        for (int i = 0; i < 8; ++i) { const float cv = __uint_as_float((unsigned)c[rg + 64 * i] << 16); s[i] = rbf(cv / (1.f + __expf(-cv))); }
        float acc[8];
#pragma unroll
        for (int e = 0; e < 8; ++e) acc[e] = 0.f;
#pragma unroll
        for (int i = 0; i < 8; ++i)
#pragma unroll
            for (int e = 0; e < 4; ++e) { acc[2 * e] += s[i] * bflo(w[i][e]); acc[2 * e + 1] += s[i] * bfhi(w[i][e]); }
        float* red = (float*)smem;
        *(f32x4*)(red + rg * 36 + cq * 8) = (f32x4){acc[0], acc[1], acc[2], acc[3]};
        *(f32x4*)(red + rg * 36 + cq * 8 + 4) = (f32x4){acc[4], acc[5], acc[6], acc[7]};
        __syncthreads();
        const int col = tid >> 3, part = tid & 7;
        float v = 0.f;
#pragma unroll
        for (int i = 0; i < 8; ++i) v += red[(part * 8 + i) * 36 + col];
        v += __shfl_xor(v, 1); v += __shfl_xor(v, 2); v += __shfl_xor(v, 4);
        if (part == 0) {
            const int cc = c0 + col, sec = cc >> 9;
            float r = rbf(v);
            if (sec == 1 || sec == 4) r = rbf(1.f + r);
            ((float*)(ws + WS_MOD))[cc] = r;
        }
        return;
    }
    b -= K1_MOD;
    if (b < K1_WQ + K1_WKV + K1_WO) {
        if (b >= K1_WQ && b < K1_WQ + K1_WKV) {
            const int j = b - K1_WQ, r0 = j * 128;
#pragma unroll
            for (int i = 0; i < 2; ++i) {
                const int it = tid + 256 * i, row = it >> 2, m = (it >> 1) & 1, ch = it & 1;
                const u32x4 v = *(const u32x4*)((m ? wv : wk) + (size_t)(r0 + row) * 16 + ch * 8);
                *(u32x4*)(smem + row * 80 + m * 32 + ch * 16) = v;
            }
            __syncthreads();
#pragma unroll
            for (int m = 0; m < 2; ++m) {
                const int ks = wid;
                const s16x4 lo = trread(lds + (ks * 32 + 8 * grp + q) * 80 + m * 32 + p * 8);
                const s16x4 hi = trread(lds + (ks * 32 + 8 * grp + 4 + q) * 80 + m * 32 + p * 8);
                *(bf16x8*)(ws + WS_WQKV + ((size_t)(32 + m) * 16 + j * 4 + ks) * 1024 + lane * 16) = __builtin_shufflevector(lo, hi, 0, 1, 2, 3, 4, 5, 6, 7);
            }
            return;
        }
        const bool isq = b < K1_WQ;
        const int j = isq ? b : b - K1_WQ - K1_WKV;
        const uint16_t* src = isq ? wq : wo;
        const int kk = j >> 2, cg = j & 3;
#pragma unroll
        for (int i = 0; i < 2; ++i) {
            const int it = tid + 256 * i, row = it >> 4, ch = it & 15;
            const u32x4 v = *(const u32x4*)(src + (size_t)(kk * 32 + row) * 512 + cg * 128 + ch * 8);
            *(u32x4*)(smem + row * 272 + ch * 16) = v;
        }
        __syncthreads();
#pragma unroll
        for (int i = 0; i < 2; ++i) {
            const int nl = wid * 2 + i, nb = cg * 8 + nl;
            const s16x4 lo = trread(lds + (8 * grp + q) * 272 + nl * 32 + p * 8);
            const s16x4 hi = trread(lds + (8 * grp + 4 + q) * 272 + nl * 32 + p * 8);
            size_t dst;
            if (isq) dst = WS_WQKV + ((size_t)nb * 16 + kk) * 1024;
            else dst = WS_WO + ((((size_t)(kk >> 1) * 8 + (nb >> 2)) * 4 + (nb & 3)) * 2 + (kk & 1)) * 1024;
            *(bf16x8*)(ws + dst + lane * 16) = __builtin_shufflevector(lo, hi, 0, 1, 2, 3, 4, 5, 6, 7);
        }
        return;
    }
    b -= K1_WQ + K1_WKV + K1_WO;
    if (b < K1_WGU) {
        const int kk = b >> 3, fq = b & 7;
        constexpr int PITCH = 896 + 16;
        u32x4 g[4], u[4];
#pragma unroll
        for (int i = 0; i < 4; ++i) {
            const int it = tid + 256 * i;
            if (it < 896) {
                const int row = it / 28, ch = it - row * 28;
                const size_t off = (size_t)(kk * 32 + row) * FF + fq * 224 + ch * 8;
                g[i] = *(const u32x4*)(wg + off); u[i] = *(const u32x4*)(wu + off);
            }
        }
#pragma unroll
        for (int i = 0; i < 4; ++i) {
            const int it = tid + 256 * i;
            if (it < 896) {
                const int row = it / 28, ch = it - row * 28;
                *(u32x4*)(smem + row * PITCH + ch * 32) = (u32x4){g[i][0], u[i][0], g[i][1], u[i][1]};
                *(u32x4*)(smem + row * PITCH + ch * 32 + 16) = (u32x4){g[i][2], u[i][2], g[i][3], u[i][3]};
            }
        }
        __syncthreads();
#pragma unroll
        for (int i = 0; i < 7; ++i) {
            const int ch = wid * 7 + i;
            const s16x4 lo = trread(lds + (8 * grp + q) * PITCH + ch * 32 + p * 8);
            const s16x4 hi = trread(lds + (8 * grp + 4 + q) * PITCH + ch * 32 + p * 8);
            *(bf16x8*)(ws + WS_WGU + (((size_t)(fq * 4 + wid) * 16 + kk) * 7 + i) * 1024 + lane * 16) = __builtin_shufflevector(lo, hi, 0, 1, 2, 3, 4, 5, 6, 7);
        }
        return;
    }
    b -= K1_WGU;
    {
        const int P = b >> 2, cg = b & 3;
        u32x4 v[7];
#pragma unroll
        for (int i = 0; i < 7; ++i) { const int it = tid + 256 * i, row = it >> 4, ch = it & 15; v[i] = *(const u32x4*)(wd + (size_t)(P * 112 + row) * 512 + cg * 128 + ch * 8); }
#pragma unroll
        for (int i = 0; i < 7; ++i) { const int it = tid + 256 * i, row = it >> 4, ch = it & 15; *(u32x4*)(smem + row * 272 + ch * 16) = v[i]; }
        __syncthreads();
        const int h = grp >> 1, cb = wid * 32 + (grp & 1) * 16;
#pragma unroll
        for (int kap = 0; kap < 7; ++kap) {
            const int sg = 2 * kap + h;
            const s16x4 lo = trread(lds + wd_row(sg, q) * 272 + (cb + 4 * p) * 2);
            const s16x4 hi = trread(lds + wd_row(sg, 4 + q) * 272 + (cb + 4 * p) * 2);
            const int n32 = cg * 4 + wid;
            *(bf16x8*)(ws + WS_WD + (((size_t)(n32 >> 1) * 112 + P * 7 + kap) * 2 + (n32 & 1)) * 1024 + lane * 16) = __builtin_shufflevector(lo, hi, 0, 1, 2, 3, 4, 5, 6, 7);
        }
    }
}

__global__ __launch_bounds__(512, 2) void k2_qkv(const uint16_t* __restrict__ x, char* __restrict__ ws) {
    __shared__ __attribute__((aligned(16))) char smem[32768];
    const int tid = threadIdx.x, lane = tid & 63, wid = __builtin_amdgcn_readfirstlane(tid >> 6);
    const int b = blockIdx.x, xcd = b & 7, idx = b >> 3, rt = xcd * 8 + (idx >> 2), cg = idx & 3;
    const int t0 = rt * 32, l16 = lane & 15, grp = lane >> 4;
    const int tok = wid * 4 + grp;
    u32x4 xv[4];
#pragma unroll
    for (int i = 0; i < 4; ++i) xv[i] = *(const u32x4*)(x + (size_t)(t0 + tok) * DM + (i * 16 + l16) * 8);
    const float* mod = (const float*)(ws + WS_MOD);
    f32x4 sa[8], ca[8];
#pragma unroll
    for (int i = 0; i < 4; ++i) {
        const int c = (i * 16 + l16) * 8;
        sa[2 * i] = *(const f32x4*)(mod + c); sa[2 * i + 1] = *(const f32x4*)(mod + c + 4);
        ca[2 * i] = *(const f32x4*)(mod + 512 + c); ca[2 * i + 1] = *(const f32x4*)(mod + 512 + c + 4);
    }
    u32x4 wf[16];
    const char* wsrc = ws + WS_WQKV + (size_t)(cg * 8 + wid) * 16384 + lane * 16;
#pragma unroll
    for (int k = 0; k < 16; ++k) wf[k] = *(const u32x4*)(wsrc + k * 1024);
    float v[32];
#pragma unroll
    for (int i = 0; i < 4; ++i)
#pragma unroll
        for (int e = 0; e < 4; ++e) { v[i * 8 + 2 * e] = bflo(xv[i][e]); v[i * 8 + 2 * e + 1] = bfhi(xv[i][e]); }
    float s = 0.f;
#pragma unroll
    for (int i = 0; i < 32; ++i) s += v[i];
    const float mean = row16_sum(s) * (1.f / 512.f);
    float ss = 0.f;
#pragma unroll
    for (int i = 0; i < 32; ++i) { v[i] -= mean; ss += v[i] * v[i]; }
    const float rstd = __builtin_amdgcn_rsqf(row16_sum(ss) * (1.f / 512.f) + 1e-5f);
    const int mbw = tok >> 4, t16 = tok & 15;
#pragma unroll
    for (int i = 0; i < 4; ++i) {
        float h[8];
#pragma unroll
        for (int e = 0; e < 8; ++e) h[e] = v[i * 8 + e] * rstd * ca[2 * i + (e >> 2)][e & 3] + sa[2 * i + (e >> 2)][e & 3];
        const int c8 = i * 16 + l16, ks = c8 >> 2, g = c8 & 3;
        *(u32x4*)(smem + ((mbw * 16 + ks) * 64 + g * 16 + (t16 ^ (g | ((ks & 1) << 2)))) * 16) =
            (u32x4){cvtpk(h[0], h[1]), cvtpk(h[2], h[3]), cvtpk(h[4], h[5]), cvtpk(h[6], h[7])};
    }
    u32x4 we[16];
    if (wid == 0) {
        const char* esrc = ws + WS_WQKV + (size_t)(32 + (cg >> 1)) * 16384 + lane * 16;
#pragma unroll
        for (int k = 0; k < 16; ++k) we[k] = *(const u32x4*)(esrc + k * 1024);
    }
    __syncthreads();
    f32x4 acc0 = {0.f, 0.f, 0.f, 0.f}, acc1 = acc0;
    const char* hb0 = smem + (grp * 16 + (l16 ^ grp)) * 16;
    const char* hb1 = smem + (grp * 16 + (l16 ^ (grp | 4))) * 16;
#pragma unroll
    for (int k = 0; k < 16; ++k) {
        const char* hp = (k & 1) ? hb1 : hb0;
        const u32x4 h0 = *(const u32x4*)(hp + k * 1024), h1 = *(const u32x4*)(hp + (16 + k) * 1024);
        acc0 = mfma16(wf[k], h0, acc0); acc1 = mfma16(wf[k], h1, acc1);
    }
    constexpr float QS = 0.25f * 1.4426950408889634f;
    uint16_t* qo = (uint16_t*)(ws + WS_Q) + (size_t)(t0 + l16) * DM + (cg * 8 + wid) * 16 + grp * 4;
    *(u32x2*)qo = (u32x2){cvtpk(acc0[0] * QS, acc0[1] * QS), cvtpk(acc0[2] * QS, acc0[3] * QS)};
    *(u32x2*)(qo + 16 * DM) = (u32x2){cvtpk(acc1[0] * QS, acc1[1] * QS), cvtpk(acc1[2] * QS, acc1[3] * QS)};
    if (wid == 0) {
        const int mb = cg & 1;
        f32x4 a = {0.f, 0.f, 0.f, 0.f};
        if (cg < 2) {
#pragma unroll
            for (int k = 0; k < 16; ++k) { const char* hp = (k & 1) ? hb1 : hb0; a = mfma16(we[k], *(const u32x4*)(hp + (mb * 16 + k) * 1024), a); }
            const u32x2 pk = {cvtpk(a[0], a[1]), cvtpk(a[2], a[3])};
            *(u32x2*)(ws + WS_KT + (size_t)rt * 1024 + ((grp >> 1) * 32 + mb * 16 + l16) * 16 + (grp & 1) * 8) = pk;
            float n2 = bflo(pk[0]) * bflo(pk[0]) + bfhi(pk[0]) * bfhi(pk[0]) + bflo(pk[1]) * bflo(pk[1]) + bfhi(pk[1]) * bfhi(pk[1]);
            n2 = swap32_sum(swap16_sum(n2));
            n2 = row16_max(n2);
            if (lane == 0) ((float*)(ws + WS_KMAX))[rt * 2 + mb] = n2;
        } else {
#pragma unroll
            for (int k = 0; k < 16; ++k) { const char* hp = (k & 1) ? hb1 : hb0; a = mfma16(*(const u32x4*)(hp + (mb * 16 + k) * 1024), we[k], a); }
            *(u32x2*)(ws + WS_VT + (size_t)rt * 1024 + mb * 512 + ((grp & 1) * 16 + l16) * 16 + (grp >> 1) * 8) = (u32x2){cvtpk(a[0], a[1]), cvtpk(a[2], a[3])};
        }
    }
}

constexpr int A_QF = 0;
constexpr int A_CB = 8192;
constexpr int A_PART = A_CB + 4096;
constexpr int A_OB = A_PART + 8 * 9 * 256;
constexpr int A_ST = A_OB + 2048;
constexpr int A_MX = A_ST + 512;
constexpr int A_FLAG = A_MX + 1024;
constexpr int A_BYTES = A_FLAG + 16;

struct AttnState { u32x4 kf[8]; u32x4 vf[8][2]; u32x4 wo[8]; f32x4 xacc[4]; };

DI void attn_hookA(float (&pr)[16], const char* smem, int wid, int lane) {
    LDS_BAR();
    const float* part = (const float*)(smem + A_PART);
#pragma unroll
    for (int w = 0; w < 8; ++w) { pr[2 * w] = part[(w * 9 + wid) * 64 + lane]; pr[2 * w + 1] = part[(w * 9 + 8) * 64 + lane]; }
}
DI void attn_hookB(const float (&pr)[16], char* smem, int wid, int lane) {
    const float o = ((pr[0] + pr[2]) + (pr[4] + pr[6])) + ((pr[8] + pr[10]) + (pr[12] + pr[14]));
    const float l = ((pr[1] + pr[3]) + (pr[5] + pr[7])) + ((pr[9] + pr[11]) + (pr[13] + pr[15]));
    if (!(l >= 0x1p-90f)) *(volatile LAS int*)(smem + A_FLAG) = 1;
    const float r = o * __builtin_amdgcn_rcpf(l);
    const int q = lane & 31, h = lane >> 5, hh = q >> 3, tt = q & 7;
    const int off = (hh >> 1) * 1024 + (((hh & 1) * 2 + (wid >> 2)) * 16 + tt) * 16 + ((wid & 3) + 4 * h) * 2;
    *(uint16_t*)(smem + A_OB + off) = (uint16_t)cvtpk(r, r);
}
DI void attn_hookC(u32x4 (&of)[2], const char* smem, int lane) {
    LDS_BAR();
    const char* p = smem + A_OB + ((lane & 48) + (lane & 7)) * 16;
    of[0] = *(const u32x4*)p; of[1] = *(const u32x4*)(p + 1024);
}
DI void attn_hookD(AttnState& st, const u32x4 (&of)[2]) {
#pragma unroll
    for (int cb = 0; cb < 4; ++cb) { st.xacc[cb] = mfma16(st.wo[2 * cb], of[0], st.xacc[cb]); st.xacc[cb] = mfma16(st.wo[2 * cb + 1], of[1], st.xacc[cb]); }
}
DI void attn_loadwo(AttnState& st, const char* wop, int j) {
#pragma unroll
    for (int i = 0; i < 8; ++i) st.wo[i] = *(const u32x4*)(wop + (size_t)j * 65536 + i * 1024);
}

template <bool HOOKS>
DI void attn_phase(AttnState& st, char* smem, const char* wop, int j, int wid, int lane) {
    const u32x4 qf = *(const u32x4*)(smem + A_QF + j * 1024 + lane * 16);
    const f32x4 c4 = *(const f32x4*)(smem + A_CB + j * 512 + (lane & 31) * 16);
    f32x16 cn;
    {
        const volatile LAS f32x4* cp = (const volatile LAS f32x4*)(smem + A_CB + j * 512 + (lane & 31) * 16);
        const f32x4 c1 = *cp, c2 = *cp, c3 = *cp;
        cn = (f32x16){c4[0], c4[1], c4[2], c4[3], c1[0], c1[1], c1[2], c1[3], c2[0], c2[1], c2[2], c2[3], c3[0], c3[1], c3[2], c3[3]};
    }
    f32x16 s[2], o;
    float pr[16]; u32x4 of[2];
    s[0] = mfma32(st.kf[0], qf, cn);
#pragma unroll
    for (int t = 0; t < 8; ++t) {
        f32x16& sc = s[t & 1];
        u32x4 p0, p1;
        sc[0] = __builtin_amdgcn_exp2f(sc[0]); sc[1] = __builtin_amdgcn_exp2f(sc[1]); sc[2] = __builtin_amdgcn_exp2f(sc[2]); sc[3] = __builtin_amdgcn_exp2f(sc[3]);
        p0[0] = cvtpk(sc[0], sc[1]);
        SBAR();
        if (t < 7) s[(t + 1) & 1] = mfma32(st.kf[t + 1], qf, cn);
        sc[4] = __builtin_amdgcn_exp2f(sc[4]); sc[5] = __builtin_amdgcn_exp2f(sc[5]); p0[1] = cvtpk(sc[2], sc[3]);
        sc[6] = __builtin_amdgcn_exp2f(sc[6]); sc[7] = __builtin_amdgcn_exp2f(sc[7]); p0[2] = cvtpk(sc[4], sc[5]);
        sc[8] = __builtin_amdgcn_exp2f(sc[8]); p0[3] = cvtpk(sc[6], sc[7]);
        sc[9] = __builtin_amdgcn_exp2f(sc[9]); sc[10] = __builtin_amdgcn_exp2f(sc[10]);
        SBAR();
        if (t == 0) { const f32x16 z = {}; o = mfma32(st.vf[t][0], p0, z); } else o = mfma32(st.vf[t][0], p0, o);
        sc[11] = __builtin_amdgcn_exp2f(sc[11]); p1[0] = cvtpk(sc[8], sc[9]);
        sc[12] = __builtin_amdgcn_exp2f(sc[12]); sc[13] = __builtin_amdgcn_exp2f(sc[13]); p1[1] = cvtpk(sc[10], sc[11]);
        sc[14] = __builtin_amdgcn_exp2f(sc[14]); sc[15] = __builtin_amdgcn_exp2f(sc[15]); p1[2] = cvtpk(sc[12], sc[13]);
        p1[3] = cvtpk(sc[14], sc[15]);
        SBAR();
        o = mfma32(st.vf[t][1], p1, o);
        SBAR();
        if (HOOKS) {
            if (t == 0) attn_hookA(pr, smem, wid, lane);
            if (t == 1) { attn_hookB(pr, smem, wid, lane); SBAR(); attn_loadwo(st, wop, j - 1); }
            if (t == 5) attn_hookC(of, smem, lane);
            if (t == 6) attn_hookD(st, of);
            if (t == 7 && j == 7) attn_loadwo(st, wop, 7);
            SBAR();
        }
    }
    float* part = (float*)(smem + A_PART) + wid * 9 * 64 + lane;
#pragma unroll
    for (int r = 0; r < 9; ++r) part[r * 64] = o[r];
}

__global__ __launch_bounds__(512, 2) void k3_attn(const uint16_t* __restrict__ x, char* __restrict__ ws) {
    __shared__ __attribute__((aligned(16))) char smem[A_BYTES];
    const int tid = threadIdx.x, lane = tid & 63, wid = __builtin_amdgcn_readfirstlane(tid >> 6);
    const int b = blockIdx.x, t0 = ((b & 7) * 32 + (b >> 3)) * 8;
    const int q32 = lane & 31, hf = lane >> 5;
    AttnState st;
    const u32x4 qv = *(const u32x4*)(ws + WS_Q + (size_t)(t0 + (q32 & 7)) * 1024 + (wid * 4 + (q32 >> 3)) * 32 + hf * 16);
    const f32x2 km = *(const f32x2*)(ws + WS_KMAX + lane * 8);
#pragma unroll
    for (int t = 0; t < 8; ++t) st.kf[t] = *(const u32x4*)(ws + WS_KT + (size_t)(wid * 8 + t) * 1024 + lane * 16);
    const u32x4 ones = {0x3f803f80u, 0x3f803f80u, 0x3f803f80u, 0x3f803f80u};
#pragma unroll
    for (int t = 0; t < 8; ++t) { st.vf[t][0] = ones; st.vf[t][1] = ones; }
    if (q32 < 16) {
        const char* vp = ws + WS_VT + (size_t)wid * 8192 + (hf * 16 + q32) * 16;
#pragma unroll
        for (int t = 0; t < 8; ++t) { st.vf[t][0] = *(const u32x4*)(vp + t * 1024); st.vf[t][1] = *(const u32x4*)(vp + t * 1024 + 512); }
    }
    const char* wop = ws + WS_WO + (size_t)wid * 8192 + lane * 16;
    {
        float k2 = fmaxf(km[0], km[1]);
        k2 = swap32_max(swap16_max(row16_max(k2)));
        float n2 = 0.f;
#pragma unroll
        for (int e = 0; e < 4; ++e) { n2 += bflo(qv[e]) * bflo(qv[e]); n2 += bfhi(qv[e]) * bfhi(qv[e]); }
        n2 = swap32_sum(n2);
        const float nb = -__builtin_amdgcn_sqrtf(n2 * k2);
        *(u32x4*)(smem + A_QF + wid * 1024 + lane * 16) = qv;
        if (hf == 0) *(f32x4*)(smem + A_CB + wid * 512 + q32 * 16) = (f32x4){nb, nb, nb, nb};
        if (tid == 0) *(int*)(smem + A_FLAG) = 0;
    }
    LDS_BAR();
    for (int attempt = 0;; ++attempt) {
#pragma unroll
        for (int cb = 0; cb < 4; ++cb) st.xacc[cb] = (f32x4){0.f, 0.f, 0.f, 0.f};
        attn_phase<false>(st, smem, wop, 0, wid, lane);
        for (int j = 1; j < 8; ++j) attn_phase<true>(st, smem, wop, j, wid, lane);
        {
            float pr[16]; u32x4 of[2];
            attn_hookA(pr, smem, wid, lane);
            attn_hookB(pr, smem, wid, lane);
            attn_hookC(of, smem, lane);
            attn_hookD(st, of);
        }
        const int flag = __builtin_amdgcn_readfirstlane(*(volatile LAS int*)(smem + A_FLAG));
        if (flag == 0 || attempt == 1) break;
        for (int j = 0; j < 8; ++j) {
            const u32x4 qf = *(const u32x4*)(smem + A_QF + j * 1024 + lane * 16);
            float m = -INFINITY;
#pragma unroll
            for (int t = 0; t < 8; ++t) {
                const f32x16 z = {};
                const f32x16 sv = mfma32(st.kf[t], qf, z);
#pragma unroll
                for (int r = 0; r < 16; ++r) m = fmaxf(m, sv[r]);
            }
            m = swap32_max(m);
            LDS_BAR();
            if (hf == 0) ((float*)(smem + A_MX))[wid * 32 + q32] = m;
            LDS_BAR();
            if (wid == 0 && hf == 0) {
                float mm = -INFINITY;
#pragma unroll
                for (int w = 0; w < 8; ++w) mm = fmaxf(mm, ((const float*)(smem + A_MX))[w * 32 + q32]);
                *(f32x4*)(smem + A_CB + j * 512 + q32 * 16) = (f32x4){-mm, -mm, -mm, -mm};
            }
        }
        LDS_BAR();
    }
    const int n = lane & 7, g = lane >> 4, up = (lane >> 3) & 1;
    const float* mod = (const float*)(ws + WS_MOD);
    float xv[8];
    const int cbase = wid * 64 + up * 32 + g * 4;
#pragma unroll
    for (int i = 0; i < 2; ++i) {
        const int c = cbase + i * 16;
        const u32x2 xr = *(const u32x2*)(x + (size_t)(t0 + n) * DM + c);
        const f32x4 ga = *(const f32x4*)(mod + 1024 + c);
        const f32x4 a = up ? st.xacc[2 + i] : st.xacc[i];
        const unsigned w0 = cvtpk(bflo(xr[0]) + ga[0] * a[0], bfhi(xr[0]) + ga[1] * a[1]);
        const unsigned w1 = cvtpk(bflo(xr[1]) + ga[2] * a[2], bfhi(xr[1]) + ga[3] * a[3]);
        *(u32x2*)(ws + WS_X2 + ((size_t)(t0 + n) * DM + c) * 2) = (u32x2){w0, w1};
        xv[4 * i] = bflo(w0); xv[4 * i + 1] = bfhi(w0); xv[4 * i + 2] = bflo(w1); xv[4 * i + 3] = bfhi(w1);
    }
    float s = ((xv[0] + xv[1]) + (xv[2] + xv[3])) + ((xv[4] + xv[5]) + (xv[6] + xv[7]));
    s = swap32_sum(swap16_sum(dpp_add<0x128>(s)));
    const float mw = s * (1.f / 64.f);
    float m2 = 0.f;
#pragma unroll
    for (int i = 0; i < 8; ++i) m2 += (xv[i] - mw) * (xv[i] - mw);
    m2 = swap32_sum(swap16_sum(dpp_add<0x128>(m2)));
    if (lane < 8) *(f32x2*)(smem + A_ST + (n * 8 + wid) * 8) = (f32x2){mw, m2};
    LDS_BAR();
    f32x4 sv[4];
#pragma unroll
    for (int i = 0; i < 4; ++i) sv[i] = *(const f32x4*)(smem + A_ST + n * 64 + i * 16);
    const float mean = (((sv[0][0] + sv[0][2]) + (sv[1][0] + sv[1][2])) + ((sv[2][0] + sv[2][2]) + (sv[3][0] + sv[3][2]))) * 0.125f;
    float M2 = 0.f;
#pragma unroll
    for (int i = 0; i < 4; ++i) { const float d0 = sv[i][0] - mean, d1 = sv[i][2] - mean; M2 += sv[i][1] + sv[i][3] + 64.f * (d0 * d0 + d1 * d1); }
    const float rstd = __builtin_amdgcn_rsqf(M2 * (1.f / 512.f) + 1e-5f);
    const int tok = t0 + n;
#pragma unroll
    for (int i = 0; i < 2; ++i) {
        const int c = cbase + i * 16;
        const f32x4 sb = *(const f32x4*)(mod + 1536 + c), cb = *(const f32x4*)(mod + 2048 + c);
        float h[4];
#pragma unroll
        for (int r = 0; r < 4; ++r) h[r] = (xv[4 * i + r] - mean) * rstd * cb[r] + sb[r];
        const int ks = c >> 5, gp = (c >> 3) & 3;
        *(u32x2*)(ws + WS_H2 + ((size_t)(tok >> 4) * 16 + ks) * 1024 + (gp * 16 + (tok & 15)) * 16 + (g & 1) * 8) = (u32x2){cvtpk(h[0], h[1]), cvtpk(h[2], h[3])};
    }
}

DI void glds16(const void* g, unsigned lds_base) {
    unsigned sv;
    asm volatile("s_mov_b32 %0, m0\n\ts_mov_b32 m0, %2\n\ts_nop 0\n\tglobal_load_lds_dwordx4 %1, off\n\ts_mov_b32 m0, %0" : "=&s"(sv) : "v"(g), "s"(lds_base) : "memory");
}
DI float geglu(float gt, float up) {
    constexpr float C1 = -2.f * 0.7978845608028654f * 1.4426950408889634f, C2 = C1 * 0.044715f;
    const float z = gt * (C1 + C2 * gt * gt);
    return gt * up * __builtin_amdgcn_rcpf(1.f + __builtin_amdgcn_exp2f(z));
}

__global__ __launch_bounds__(512, 2) void k5_ffn1(char* __restrict__ ws) {
    extern __shared__ __attribute__((aligned(16))) char dsm[];
    const int tid = threadIdx.x, lane = tid & 63, wid = __builtin_amdgcn_readfirstlane(tid >> 6);
    const int mt = blockIdx.x & 7, ft = blockIdx.x >> 3;
    const unsigned lds0 = (unsigned)(uintptr_t)dsm;
    const int wb = wid < 7 ? wid : 6;
    const char* wsrc = ws + WS_WGU + (size_t)ft * (16 * 7 * 1024) + wb * 1024 + lane * 16;
    const char* hsrc = ws + WS_H2 + (size_t)(mt * 16 + wid * 2) * 16384 + lane * 16;
    u32x4 hb[16][2];
#pragma unroll
    for (int k = 0; k < 16; ++k) {
        glds16(wsrc + k * 7168, lds0 + k * 7168 + wb * 1024);
        asm volatile("global_load_dwordx4 %0, %1, off" : "=v"(hb[k][0]) : "v"(hsrc + k * 1024) : "memory");
        asm volatile("global_load_dwordx4 %0, %1, off" : "=v"(hb[k][1]) : "v"(hsrc + 16384 + k * 1024) : "memory");
    }
    f32x4 acc[7][2];
#pragma unroll
    for (int i = 0; i < 7; ++i) { acc[i][0] = (f32x4){0.f, 0.f, 0.f, 0.f}; acc[i][1] = acc[i][0]; }
    const char* wl = dsm + lane * 16;
#define K5_STEP(k) do { _Pragma("unroll") for (int i = 0; i < 7; ++i) { const u32x4 wf = *(const u32x4*)(wl + (k) * 7168 + i * 1024); \
        acc[i][0] = mfma16(wf, hb[k][0], acc[i][0]); acc[i][1] = mfma16(wf, hb[k][1], acc[i][1]); } } while (0)
#define K5_CHUNK(k, N) do { asm volatile("s_waitcnt vmcnt(" #N ")\n\ts_barrier" : "+v"(hb[k][0]), "+v"(hb[k][1]), "+v"(hb[k + 1][0]), "+v"(hb[k + 1][1]) :: "memory"); \
        K5_STEP(k); K5_STEP(k + 1); } while (0)
    K5_CHUNK(0, 42); K5_CHUNK(2, 36); K5_CHUNK(4, 30); K5_CHUNK(6, 24); K5_CHUNK(8, 18); K5_CHUNK(10, 12); K5_CHUNK(12, 6); K5_CHUNK(14, 0);
    const int l16 = lane & 15, g = lane >> 4;
    char* abase = ws + WS_ACT + ((size_t)(mt * 8 + wid) * 112 + (ft >> 1) * 7) * 1024;
    const int sg0 = 7 * (ft & 1);
#pragma unroll
    for (int mb = 0; mb < 2; ++mb) {
        unsigned d[7];
#pragma unroll
        for (int i = 0; i < 7; ++i) d[i] = cvtpk(geglu(acc[i][mb][0], acc[i][mb][2]), geglu(acc[i][mb][1], acc[i][mb][3]));
        const int tk = mb * 16 + l16;
        { const int sg = sg0 + g; *(u32x4*)(abase + (sg >> 1) * 1024 + ((sg & 1) * 32 + tk) * 16) = (u32x4){d[0], d[1], d[2], d[3]}; }
        if (g < 3) { const int sg = sg0 + 4 + g; *(u32x3*)(abase + (sg >> 1) * 1024 + ((sg & 1) * 32 + tk) * 16) = (u32x3){d[4], d[5], d[6]}; }
        else {
#pragma unroll
            for (int i = 0; i < 3; ++i) { const int sg = sg0 + 4 + i; *(unsigned*)(abase + (sg >> 1) * 1024 + ((sg & 1) * 32 + tk) * 16 + 12) = d[4 + i]; }
        }
    }
}

__global__ __launch_bounds__(256, 1) void k6_ffn2(char* __restrict__ ws, uint16_t* __restrict__ out) {
    __shared__ __attribute__((aligned(16))) char smem[4 * 3 * 4096];
    const int tid = threadIdx.x, lane = tid & 63, wid = __builtin_amdgcn_readfirstlane(tid >> 6);
    const int b = blockIdx.x, idx = b >> 3, m64 = (b & 7) * 4 + (idx >> 3), nt = idx & 7;
    const char* asrc = ws + WS_ACT + ((size_t)(m64 * 2) * 112 + wid * 28) * 1024 + lane * 16;
    const char* wsrc = ws + WS_WD + ((size_t)nt * 112 + wid * 28) * 2048 + lane * 16;
    constexpr int PF = 14;
    u32x4 af[28][2], wf[28][2];
#define K6_LOAD(k) do { af[k][0] = *(const u32x4*)(asrc + (k) * 1024); wf[k][0] = *(const u32x4*)(wsrc + (k) * 2048); \
                        af[k][1] = *(const u32x4*)(asrc + 112 * 1024 + (k) * 1024); wf[k][1] = *(const u32x4*)(wsrc + (k) * 2048 + 1024); } while (0)
#pragma unroll
    for (int k = 0; k < PF; ++k) K6_LOAD(k);
    const int bm = wid >> 1, bn = wid & 1, tk = lane & 31, hf = lane >> 5;
    const size_t orow = (size_t)(m64 * 64 + bm * 32 + tk) * DM + nt * 64 + bn * 32 + hf * 8;
    const u32x4 x2a = *(const u32x4*)(ws + WS_X2 + orow * 2), x2b = *(const u32x4*)(ws + WS_X2 + orow * 2 + 32);
    const float* gbp = (const float*)(ws + WS_MOD) + 2560 + nt * 64 + bn * 32 + hf * 8;
    const f32x4 gb0 = *(const f32x4*)gbp, gb1 = *(const f32x4*)(gbp + 4), gb2 = *(const f32x4*)(gbp + 16), gb3 = *(const f32x4*)(gbp + 20);
    SBAR();
    f32x16 acc[2][2];
#pragma unroll
    for (int k = 0; k < 28; ++k) {
        if (k == 0) {
            const f32x16 z = {};
            acc[0][0] = mfma32(wf[0][0], af[0][0], z); acc[1][0] = mfma32(wf[0][0], af[0][1], z);
            acc[0][1] = mfma32(wf[0][1], af[0][0], z); acc[1][1] = mfma32(wf[0][1], af[0][1], z);
        } else {
            acc[0][0] = mfma32(wf[k][0], af[k][0], acc[0][0]); acc[1][0] = mfma32(wf[k][0], af[k][1], acc[1][0]);
            acc[0][1] = mfma32(wf[k][1], af[k][0], acc[0][1]); acc[1][1] = mfma32(wf[k][1], af[k][1], acc[1][1]);
        }
        if (k + PF < 28) K6_LOAD(k + PF);
        SBAR();
    }
#pragma unroll
    for (int be = 0; be < 4; ++be) {
        if (be != wid) {
            const int sl = wid < be ? wid : wid - 1;
            char* p = smem + (be * 3 + sl) * 4096 + lane * 16;
            const f32x16& a = acc[be >> 1][be & 1];
#pragma unroll
            for (int qd = 0; qd < 4; ++qd) *(f32x4*)(p + qd * 1024) = (f32x4){a[4 * qd], a[4 * qd + 1], a[4 * qd + 2], a[4 * qd + 3]};
        }
    }
    f32x16 r = wid == 0 ? acc[0][0] : wid == 1 ? acc[0][1] : wid == 2 ? acc[1][0] : acc[1][1];
    __syncthreads();
#pragma unroll
    for (int sl = 0; sl < 3; ++sl)
#pragma unroll
        for (int qd = 0; qd < 4; ++qd) {
            const f32x4 v = *(const f32x4*)(smem + (wid * 3 + sl) * 4096 + qd * 1024 + lane * 16);
            r[4 * qd] += v[0]; r[4 * qd + 1] += v[1]; r[4 * qd + 2] += v[2]; r[4 * qd + 3] += v[3];
        }
    float y[16];
#pragma unroll
    for (int pr = 0; pr < 2; ++pr)
#pragma unroll
        for (int e = 0; e < 4; ++e) {
            auto sw = __builtin_amdgcn_permlane32_swap(__float_as_uint(r[8 * pr + e]), __float_as_uint(r[8 * pr + 4 + e]), false, false);
            y[8 * pr + e] = __uint_as_float(sw[0]); y[8 * pr + 4 + e] = __uint_as_float(sw[1]);
        }
    const u32x4 o0 = {cvtpk(bflo(x2a[0]) + gb0[0] * y[0], bfhi(x2a[0]) + gb0[1] * y[1]), cvtpk(bflo(x2a[1]) + gb0[2] * y[2], bfhi(x2a[1]) + gb0[3] * y[3]),
                      cvtpk(bflo(x2a[2]) + gb1[0] * y[4], bfhi(x2a[2]) + gb1[1] * y[5]), cvtpk(bflo(x2a[3]) + gb1[2] * y[6], bfhi(x2a[3]) + gb1[3] * y[7])};
    const u32x4 o1 = {cvtpk(bflo(x2b[0]) + gb2[0] * y[8], bfhi(x2b[0]) + gb2[1] * y[9]), cvtpk(bflo(x2b[1]) + gb2[2] * y[10], bfhi(x2b[1]) + gb2[3] * y[11]),
                      cvtpk(bflo(x2b[2]) + gb3[0] * y[12], bfhi(x2b[2]) + gb3[1] * y[13]), cvtpk(bflo(x2b[3]) + gb3[2] * y[14], bfhi(x2b[3]) + gb3[3] * y[15])};
    *(u32x4*)(out + orow) = o0;
    *(u32x4*)(out + orow + 16) = o1;
}

extern "C" void kernel_launch(void* const* d_in, const int* in_sizes, int n_in, void* d_out, int out_size, void* d_ws, size_t ws_size, hipStream_t stream) {
    const uint16_t* x = (const uint16_t*)d_in[0];
    const uint16_t* wq = (const uint16_t*)d_in[1];
    const uint16_t* wk = (const uint16_t*)d_in[2];
    const uint16_t* wv = (const uint16_t*)d_in[3];
    const uint16_t* wo = (const uint16_t*)d_in[4];
    const uint16_t* wg = (const uint16_t*)d_in[6];
    const uint16_t* wu = (const uint16_t*)d_in[7];
    const uint16_t* wd = (const uint16_t*)d_in[8];
    const uint16_t* c = (const uint16_t*)d_in[9];
    const uint16_t* wmod = (const uint16_t*)d_in[10];
    char* ws = (char*)d_ws;
    (void)hipFuncSetAttribute((const void*)k5_ffn1, hipFuncAttributeMaxDynamicSharedMemorySize, 16 * 7 * 1024);
    k1_prep<<<K1_GRID, 256, 0, stream>>>(c, wmod, wq, wk, wv, wo, wg, wu, wd, ws);
    k2_qkv<<<256, 512, 0, stream>>>(x, ws);
    k3_attn<<<256, 512, 0, stream>>>(x, ws);
    k5_ffn1<<<256, 512, 16 * 7 * 1024, stream>>>(ws);
    k6_ffn2<<<256, 256, 0, stream>>>(ws, (uint16_t*)d_out);
}
```

```cpp
#include <hip/hip_runtime.h>
#include <hip/hip_bf16.h>
#include <stdint.h>

typedef __attribute__((ext_vector_type(8))) short bf16x8;
typedef __attribute__((ext_vector_type(4))) short s16x4;
typedef __attribute__((ext_vector_type(2))) float f32x2;
typedef __attribute__((ext_vector_type(4))) float f32x4;
typedef __attribute__((ext_vector_type(8))) float f32x8;
typedef __attribute__((ext_vector_type(16))) float f32x16;
typedef __attribute__((ext_vector_type(2))) unsigned u32x2;
typedef __attribute__((ext_vector_type(4))) unsigned u32x4;
typedef __attribute__((ext_vector_type(16))) unsigned u32x16;
typedef __attribute__((ext_vector_type(2))) __bf16 bf16x2_t;
typedef short v4i16_t __attribute__((ext_vector_type(4)));
typedef unsigned long long u64;
typedef __amdgpu_buffer_rsrc_t rsrc_t;
#define LAS __attribute__((address_space(3)))
#define DI __device__ __forceinline__

constexpr int SEQ = 2048, DM = 512, FF = 1792;

constexpr unsigned WS_EPOCH = 0;
constexpr int KVS = 32;
constexpr int RKV_ = 32;
constexpr int QS_ = 32;
constexpr int RM = 32, RKV = RKV_;
constexpr unsigned WS_F2 = 4096;
constexpr unsigned WS_FWD = WS_F2 + 2048 * QS_;
constexpr unsigned WS_F4 = WS_FWD + 16384;
constexpr unsigned WS_F3 = WS_F4 + 8 * 32768;
constexpr unsigned WS_FKV = WS_F3 + 32 * 8192;
constexpr unsigned WS_MOD = WS_FKV + RKV * 256 * KVS;
constexpr unsigned WS_MOD1 = WS_MOD + 16384;
constexpr unsigned WS_WO = WS_MOD1 + RM * 8192;
constexpr unsigned WS_WD = WS_WO + 512 * 1024;
constexpr unsigned WS_Q = WS_WD + FF * DM * 2;
constexpr unsigned WS_KT = WS_Q + SEQ * DM * 2;
constexpr unsigned WS_VT = WS_KT + 65536;
constexpr unsigned WS_X2 = WS_VT + 65536;
constexpr unsigned WS_H2 = WS_X2 + SEQ * DM * 2;
constexpr unsigned WS_ACT = WS_H2 + SEQ * DM * 2;
constexpr unsigned WS_END = WS_ACT + SEQ * FF * 2;

constexpr int L_WQ = 0, L_WM = 114688, L_WKV = 131072, L_SILU = 147456, L_MODF = 148480, L_WOR = 152576, L_H = 0;
constexpr int L_MOD2 = 114688, L_ST = 120832, L_FLAG = 121344, L_MX = 121856, L_PART = 122880, L_OB = 159744;
constexpr int LDS_BYTES = 163840;

DI unsigned cvtpk(float lo, float hi) { f32x2 v = {lo, hi}; bf16x2_t b = __builtin_convertvector(v, bf16x2_t); return __builtin_bit_cast(unsigned, b); }
DI float bflo(unsigned u) { return __uint_as_float(u << 16); }
DI float bfhi(unsigned u) { return __uint_as_float(u & 0xffff0000u); }
DI f32x2 bf2(unsigned u) { return (f32x2){bflo(u), bfhi(u)}; }
DI float rbf(float x) { return bflo(cvtpk(x, x)); }
DI s16x4 trread(const LAS char* p) { return __builtin_bit_cast(s16x4, __builtin_amdgcn_ds_read_tr16_b64_v4i16((LAS v4i16_t*)p)); }
DI u32x4 trfrag(const LAS char* p, int off2) {
    const s16x4 lo = trread(p), hi = trread(p + off2);
    return __builtin_bit_cast(u32x4, __builtin_shufflevector(lo, hi, 0, 1, 2, 3, 4, 5, 6, 7));
}
DI f32x4 mfma16(u32x4 a, u32x4 b, f32x4 c) { return __builtin_amdgcn_mfma_f32_16x16x32_bf16(__builtin_bit_cast(bf16x8, a), __builtin_bit_cast(bf16x8, b), c, 0, 0, 0); }
DI f32x16 mfma32(u32x4 a, u32x4 b, f32x16 c) { return __builtin_amdgcn_mfma_f32_32x32x16_bf16(__builtin_bit_cast(bf16x8, a), __builtin_bit_cast(bf16x8, b), c, 0, 0, 0); }
template <int CTRL> DI float dpp_add(float v) { return v + __uint_as_float(__builtin_amdgcn_update_dpp(0, __float_as_uint(v), CTRL, 0xf, 0xf, true)); }
DI float row16_sum(float v) { v = dpp_add<0x128>(v); v = dpp_add<0x124>(v); v = dpp_add<0x122>(v); v = dpp_add<0x121>(v); return v; }
DI float swap32_sum(float v) { auto r = __builtin_amdgcn_permlane32_swap(__float_as_uint(v), __float_as_uint(v), false, false); return __uint_as_float(r[0]) + __uint_as_float(r[1]); }
DI float swap16_sum(float v) { auto r = __builtin_amdgcn_permlane16_swap(__float_as_uint(v), __float_as_uint(v), false, false); return __uint_as_float(r[0]) + __uint_as_float(r[1]); }
DI float sum4rows(float a, float b, float c, float d) {
    const auto p = __builtin_amdgcn_permlane32_swap(__float_as_uint(a), __float_as_uint(b), false, false), q = __builtin_amdgcn_permlane32_swap(__float_as_uint(c), __float_as_uint(d), false, false);
    const auto r = __builtin_amdgcn_permlane16_swap(__float_as_uint(__uint_as_float(p[0]) + __uint_as_float(p[1])), __float_as_uint(__uint_as_float(q[0]) + __uint_as_float(q[1])), false, false);
    return row16_sum(__uint_as_float(r[0]) + __uint_as_float(r[1]));
}
DI float rdlane(float v, int l) { return __uint_as_float(__builtin_amdgcn_readlane(__float_as_uint(v), l)); }
DI float swap32_max(float v) { auto r = __builtin_amdgcn_permlane32_swap(__float_as_uint(v), __float_as_uint(v), false, false); return fmaxf(__uint_as_float(r[0]), __uint_as_float(r[1])); }
DI int lane_id() { int l; asm volatile("v_mbcnt_lo_u32_b32 %0, -1, 0\n\tv_mbcnt_hi_u32_b32 %0, -1, %0" : "=v"(l)); return l; }
#define LDS_BAR() do { asm volatile("s_waitcnt lgkmcnt(0)" ::: "memory"); __builtin_amdgcn_s_barrier(); asm volatile("" ::: "memory"); } while (0)
#define VM_WAIT(N) asm volatile("s_waitcnt vmcnt(" #N ")" ::: "memory")
#define SBAR() __builtin_amdgcn_sched_barrier(0)
#define PIN(x) asm volatile("" : "+v"(x))

DI void glds16(const void* g, unsigned lds_base) {
    unsigned sv;
    asm volatile("s_mov_b32 %0, m0\n\ts_mov_b32 m0, %2\n\ts_nop 0\n\tglobal_load_lds_dwordx4 %1, off\n\ts_mov_b32 m0, %0" : "=&s"(sv) : "v"(g), "s"(lds_base) : "memory");
}
template <int N> DI void glds_loop(const void* base, unsigned voff, unsigned voff2, unsigned step, unsigned lds) {
    unsigned sv, n = N / 4; u64 pb = (u64)base;
    asm volatile("s_mov_b32 %[sv], m0\n\ts_mov_b32 m0, %[lds]\n"
                 "1:\n\ts_sub_u32 %[n], %[n], 1\n\t"
                 "global_load_lds_dwordx4 %[v0], %[pb]\n\ts_add_u32 m0, m0, 1024\n\tv_add_u32 %[v0], %[st], %[v0]\n\t"
                 "global_load_lds_dwordx4 %[v0], %[pb]\n\ts_add_u32 m0, m0, 1024\n\tv_add_u32 %[v0], %[st3], %[v0]\n\t"
                 "global_load_lds_dwordx4 %[v1], %[pb]\n\ts_add_u32 m0, m0, 1024\n\tv_add_u32 %[v1], %[st], %[v1]\n\t"
                 "global_load_lds_dwordx4 %[v1], %[pb]\n\ts_add_u32 m0, m0, 1024\n\tv_add_u32 %[v1], %[st3], %[v1]\n\t"
                 "s_cmp_lg_u32 %[n], 0\n\ts_cbranch_scc1 1b\n\ts_mov_b32 m0, %[sv]"
                 : [sv] "=&s"(sv), [n] "+s"(n), [v0] "+v"(voff), [v1] "+v"(voff2) : [pb] "s"(pb), [lds] "s"(lds), [st] "s"(step), [st3] "s"(3 * step) : "scc", "memory");
}
#define GLOAD(dst, ptr) asm volatile("global_load_dwordx4 %0, %1, off" : "=v"(dst) : "v"(ptr) : "memory")
#define BLOAD1(dst, rs, voff, soff, imm) asm volatile("buffer_load_dwordx4 %0, %1, %2, %3 offen offset:" #imm " sc1" : "=v"(dst) : "v"(voff), "s"(rs), "s"(soff) : "memory")
DI void st16_sc1(rsrc_t r, unsigned off, u32x4 v) { __builtin_amdgcn_raw_buffer_store_b128(v, r, off, 0, 16); }
DI void st8_sc1(rsrc_t r, unsigned off, u64 v) { __builtin_amdgcn_raw_buffer_store_b64(__builtin_bit_cast(u32x2, v), r, off, 0, 16); }
DI u32x4 ld16_sc1(rsrc_t r, unsigned off) { return __builtin_amdgcn_raw_buffer_load_b128(r, off, 0, 16); }
constexpr unsigned SPIN_CAP = 1u << 17;
constexpr int POLL_SLEEP = 2;
DI bool tag_ok16(u32x4 v, u64 tag) { return (((u64)v[1] << 32 | v[0]) >> 16) == (tag & 0xffffffffffffull) && (((u64)v[3] << 32 | v[2]) >> 16) == (tag & 0xffffffffffffull); }
DI bool flag_ok16(u32x4 v, u64 tag) { return ((u64)v[1] << 32 | v[0]) == tag && ((u64)v[3] << 32 | v[2]) == tag; }
template <bool GRAN> DI void settle16(u32x4& v, rsrc_t r, unsigned off, u64 tag) {
    for (unsigned it = 0; !__all(GRAN ? tag_ok16(v, tag) : flag_ok16(v, tag)) && it < SPIN_CAP; ++it) { __builtin_amdgcn_s_sleep(POLL_SLEEP); v = ld16_sc1(r, off); }
    asm volatile("" ::: "memory");
}
DI void poll8(rsrc_t r, unsigned off, u64 tag) {
    for (unsigned it = 0;; ++it) {
        const u32x2 v = __builtin_amdgcn_raw_buffer_load_b64(r, off, 0, 16);
        if (__all(((u64)v[1] << 32 | v[0]) == tag) || it > SPIN_CAP) break;
        __builtin_amdgcn_s_sleep(POLL_SLEEP);
    }
    asm volatile("" ::: "memory");
}
template <int N> DI void geglu(const f32x2 (&g)[N], const f32x2 (&u)[N], unsigned (&out)[N]) {
    constexpr float C1 = -2.f * 0.7978845608028654f * 1.4426950408889634f, C2 = C1 * 0.044715f;
    f32x2 z[N], p[N];
#pragma unroll
    for (int i = 0; i < N; ++i) z[i] = g[i] * g[i];
    SBAR();
#pragma unroll
    for (int i = 0; i < N; ++i) z[i] = z[i] * C2 + C1;
    SBAR();
#pragma unroll
    for (int i = 0; i < N; ++i) z[i] *= g[i];
    SBAR();
#pragma unroll
    for (int i = 0; i < N; ++i) { z[i][0] = __builtin_amdgcn_exp2f(z[i][0]); z[i][1] = __builtin_amdgcn_exp2f(z[i][1]); p[i] = g[i] * u[i]; }
    SBAR();
#pragma unroll
    for (int i = 0; i < N; ++i) z[i] += 1.f;
    SBAR();
#pragma unroll
    for (int i = 0; i < N; ++i) { z[i][0] = __builtin_amdgcn_rcpf(z[i][0]); z[i][1] = __builtin_amdgcn_rcpf(z[i][1]); }
    SBAR();
#pragma unroll
    for (int i = 0; i < N; ++i) p[i] *= z[i];
    SBAR();
#pragma unroll
    for (int i = 0; i < N; ++i) out[i] = cvtpk(p[i][0], p[i][1]);
    SBAR();
}
DI float vmul(float a, float b) { float r; asm("v_mul_f32 %0, %1, %2" : "=v"(r) : "v"(a), "v"(b)); return r; }
template <int N> DI void geglu_s(const f32x2 (&g)[N], const f32x2 (&u)[N], unsigned (&out)[N]) {
    constexpr float C1 = -2.f * 0.7978845608028654f * 1.4426950408889634f, C2 = C1 * 0.044715f;
    float z[2 * N], p[2 * N];
#pragma unroll
    for (int i = 0; i < 2 * N; ++i) z[i] = vmul(g[i >> 1][i & 1], g[i >> 1][i & 1]);
    SBAR();
#pragma unroll
    for (int i = 0; i < 2 * N; ++i) asm("v_fma_f32 %0, %0, %1, %2" : "+v"(z[i]) : "s"(C2), "v"(C1));
    SBAR();
#pragma unroll
    for (int i = 0; i < 2 * N; ++i) z[i] = vmul(z[i], g[i >> 1][i & 1]);
    SBAR();
#pragma unroll
    for (int i = 0; i < 2 * N; ++i) { z[i] = __builtin_amdgcn_exp2f(z[i]); p[i] = vmul(g[i >> 1][i & 1], u[i >> 1][i & 1]); }
    SBAR();
    asm volatile("s_nop 0");
    SBAR();
#pragma unroll
    for (int i = 0; i < 2 * N; ++i) asm("v_add_f32 %0, 1.0, %0" : "+v"(z[i]));
    SBAR();
#pragma unroll
    for (int i = 0; i < 2 * N; ++i) z[i] = __builtin_amdgcn_rcpf(z[i]);
    SBAR();
    asm volatile("s_nop 0");
    SBAR();
#pragma unroll
    for (int i = 0; i < 2 * N; ++i) p[i] = vmul(p[i], z[i]);
    SBAR();
#pragma unroll
    for (int i = 0; i < N; ++i) out[i] = cvtpk(p[2 * i], p[2 * i + 1]);
    SBAR();
}
DI constexpr int wd_row(int s, int hi) { return s < 4 ? 4 * s + 16 * hi : 32 + 8 * (s - 4) + 4 * hi; }

DI int kv_wave(int cg) { return (cg & 1) * 4 + 3; }

__global__ __launch_bounds__(512, 2) void fused_block(const uint16_t* x, const uint16_t* wq, const uint16_t* wk, const uint16_t* wv, const uint16_t* wo, const uint16_t* wg,
                                                      const uint16_t* wu, const uint16_t* wd, const uint16_t* cnd, const uint16_t* wmod, char* ws, uint16_t* out) {
    asm volatile("" : "+s"(x), "+s"(wq), "+s"(wk), "+s"(wv), "+s"(wo), "+s"(wg), "+s"(wu), "+s"(wd), "+s"(cnd), "+s"(wmod), "+s"(ws), "+s"(out));
    extern __shared__ __attribute__((aligned(16))) char smem[];
    const LAS char* lds = (const LAS char*)smem;
    const int wid = __builtin_amdgcn_readfirstlane(threadIdx.x >> 6);
    const int b = blockIdx.x, xg = b & 7, idx = b >> 3, cw = xg * 32 + idx;
    const rsrc_t wsr = __builtin_amdgcn_make_buffer_rsrc(ws, 0, WS_END, 0x00020000);
    u64 tag;
    u32x4 g2, g3;
    {
        const int tid = threadIdx.x, lane = tid & 63, l16 = lane & 15, grp = lane >> 4, q4 = l16 >> 2, p4 = lane & 3;
        const int rt = xg * 8 + (idx >> 2), cg = idx & 3, t0 = rt * 32;
        const int mcol = ((idx >> 2) * 32 + xg * 4 + (idx & 3)) * 16;
        const int prow = (lane >> 1 & 19) | (lane >> 2 & 4) | (lane & 8);
        u32x2 ev; u32x4 cv, xv[4];
        const bool modjob = wid == 0 && idx < 24;
        if (wid == 0) __builtin_amdgcn_s_setprio(2);
        GLOAD(cv, cnd + lane * 8);
        asm volatile("global_load_dwordx2 %0, %1, off sc1" : "=v"(ev) : "v"(ws + WS_EPOCH) : "memory");
        if (wid == 7) {
            const uint16_t* src = wo + (size_t)((b >> 4) * 32 + (lane >> 2)) * DM + (b & 15) * 32 + (lane & 3) * 8;
            glds16(src, L_WOR); glds16(src + 16 * DM, L_WOR + 1024);
        }
#pragma unroll
        for (int i = 0; i < 4; ++i) GLOAD(xv[i], x + (size_t)(t0 + wid * 4 + i) * DM + lane * 8);
        if (modjob) { const unsigned o = (prow * 3072 + mcol + (lane & 1) * 8) * 2; glds_loop<16>(wmod, o, o + 2 * 32 * 6144, 32 * 6144, L_WM); }
        __builtin_amdgcn_s_barrier();
        const unsigned wq0 = (grp * DM + cg * 128 + (((l16 >> 1) ^ grp) * 2 + (lane & 1)) * 8) * 2, wq1 = (grp * DM + cg * 128 + (((l16 >> 1) ^ (grp | 4)) * 2 + (lane & 1)) * 8) * 2 + 8192;
        if (wid >= 1 && wid <= 4) glds_loop<16>(wq + (size_t)(wid - 1) * 64 * DM, wq0, wq1, 4096, L_WQ + (wid - 1) * 16384);
        if ((wid >= 1 && wid <= 4) || modjob) VM_WAIT(16); else VM_WAIT(0);
        asm volatile("" : "+v"(xv[0]), "+v"(xv[1]), "+v"(xv[2]), "+v"(xv[3]), "+v"(ev), "+v"(cv));
        tag = ((u64)(unsigned)__builtin_amdgcn_readfirstlane(ev[1]) << 32 | (u64)(unsigned)__builtin_amdgcn_readfirstlane(ev[0])) + 1;
        if (modjob) {
            unsigned sp[4];
#pragma unroll
            for (int e = 0; e < 4; ++e) {
                const float a = bflo(cv[e]), c = bfhi(cv[e]);
                sp[e] = cvtpk(a * __builtin_amdgcn_rcpf(1.f + __expf(-a)), c * __builtin_amdgcn_rcpf(1.f + __expf(-c)));
            }
            *(u32x4*)(smem + L_SILU + lane * 16) = (u32x4){sp[0], sp[1], sp[2], sp[3]};
        }
#define MOD_JOB() do { \
                u32x4 sf[16]; \
_Pragma("unroll") \
                for (int k = 0; k < 16; ++k) sf[k] = *(const u32x4*)(smem + L_SILU + k * 64 + grp * 16); \
                VM_WAIT(0); \
                const LAS char* wp = lds + L_WM + (16 * (grp >> 1) + 4 * (grp & 1) + q4) * 32 + p4 * 8; \
                u32x4 mf[16]; \
_Pragma("unroll") \
                for (int k = 0; k < 16; ++k) mf[k] = trfrag(wp + k * 1024, 256); \
                f32x4 a0 = {0.f, 0.f, 0.f, 0.f}, a1 = a0; \
_Pragma("unroll") \
                for (int k = 0; k < 16; k += 2) { a0 = mfma16(mf[k], sf[k], a0); a1 = mfma16(mf[k + 1], sf[k + 1], a1); } \
                a0 += a1; \
                float r = rbf(p4 == 0 ? a0[0] : p4 == 1 ? a0[1] : p4 == 2 ? a0[2] : a0[3]); \
                if ((mcol >> 9) == 1 || (mcol >> 9) == 4) r = 1.f + r; \
                const u64 gr = tag << 16 | (cvtpk(r, r) & 0xffffu); \
                if (mcol < 1024) { \
_Pragma("unroll") \
                    for (int i = 0; i < RM / 4; ++i) st8_sc1(wsr, WS_MOD1 + (i * 4 + q4) * 8192 + (mcol + grp * 4 + p4) * 8, gr); \
                } else if (l16 < 4) st8_sc1(wsr, WS_MOD + (mcol - 1024 + grp * 4 + l16) * 8, gr); \
            } while (0)
        f32x2 v[16];
#pragma unroll
        for (int i = 0; i < 16; ++i) v[i] = bf2(xv[i >> 2][i & 3]);
        {
            float s[4], mean[4];
#pragma unroll
            for (int t = 0; t < 4; ++t) { const f32x2 a = (v[4 * t] + v[4 * t + 1]) + (v[4 * t + 2] + v[4 * t + 3]); s[t] = a[0] + a[1]; }
            const float mv = sum4rows(s[0], s[1], s[2], s[3]) * (1.f / 512.f);
#pragma unroll
            for (int t = 0; t < 4; ++t) mean[t] = rdlane(mv, ((t >> 1) | (t & 1) << 1) * 16);
#pragma unroll
            for (int t = 0; t < 4; ++t) {
                const f32x2 d0 = v[4 * t] - mean[t], d1 = v[4 * t + 1] - mean[t], d2 = v[4 * t + 2] - mean[t], d3 = v[4 * t + 3] - mean[t];
                const f32x2 a = (d0 * d0 + d1 * d1) + (d2 * d2 + d3 * d3); s[t] = a[0] + a[1];
            }
            const float var = sum4rows(s[0], s[1], s[2], s[3]) * (1.f / 512.f);
            float mr = rbf(mv), iv = __builtin_amdgcn_rcpf(rbf(__builtin_amdgcn_sqrtf(rbf(rbf(var) + 1e-5f))));
            PIN(mr); PIN(iv);
            unsigned t16[16];
            SBAR();
            if (wid >= 5) glds_loop<16>(wq + (size_t)(wid - 1) * 64 * DM, wq0, wq1, 4096, L_WQ + (wid - 1) * 16384);
            SBAR();
#pragma unroll
            for (int i = 0; i < 16; ++i) v[i] -= rdlane(mr, ((i >> 3) | (i >> 2 & 1) << 1) * 16);
            SBAR();
#pragma unroll
            for (int i = 0; i < 16; ++i) t16[i] = cvtpk(v[i][0], v[i][1]);
            SBAR();
#pragma unroll
            for (int i = 0; i < 16; ++i) v[i] = bf2(t16[i]);
            SBAR();
#pragma unroll
            for (int i = 0; i < 16; ++i) v[i] *= rdlane(iv, ((i >> 3) | (i >> 2 & 1) << 1) * 16);
            SBAR();
#pragma unroll
            for (int i = 0; i < 16; ++i) t16[i] = cvtpk(v[i][0], v[i][1]);
            SBAR();
#pragma unroll
            for (int i = 0; i < 16; ++i) v[i] = bf2(t16[i]);
        }
#pragma unroll
        for (int i = 0; i < 16; ++i) PIN(v[i]);
        if (wid == 0) {
            if (modjob) MOD_JOB();
            glds_loop<16>(wq + (size_t)448 * DM, wq0, wq1, 4096, L_WQ + 114688);
            __builtin_amdgcn_s_setprio(0);
        }
        {
            const uint16_t* wkv = (cg < 2 ? wk : wv) + wid * 1024 + prow * 16 + (lane & 1) * 8;
            glds16(wkv, L_WKV + wid * 2048); glds16(wkv + 512, L_WKV + wid * 2048 + 1024);
        }
        u32x4 g1;
        const unsigned m1 = WS_MOD1 + (cw / (256 / RM)) * 8192;
        BLOAD1(g1, wsr, tid * 16, m1, 0);
        if (wid != 0) VM_WAIT(3);
        LDS_BAR();
        const int mb = wid >> 2, n0 = (wid & 3) * 2;
        u32x4 wf[16][2];
        const LAS char* wp0 = lds + L_WQ + (8 * grp + q4) * 256 + (n0 ^ (q4 | (grp & 1) << 2)) * 32 + p4 * 8;
        const LAS char* wp1 = lds + L_WQ + (8 * grp + q4) * 256 + ((n0 + 1) ^ (q4 | (grp & 1) << 2)) * 32 + p4 * 8;
#pragma unroll
        for (int k = 0; k < 14; ++k) { wf[k][0] = trfrag(wp0 + k * 8192, 1024); wf[k][1] = trfrag(wp1 + k * 8192, 1024); }
        if (wid == kv_wave(cg)) {
            const int kk = b >> 4;
#pragma unroll
            for (int i = 0; i < 2; ++i) {
                const int nb = (b & 15) * 2 + i;
                st16_sc1(wsr, WS_WO + ((((kk >> 1) * 8 + (nb >> 2)) * 4 + (nb & 3)) * 2 + (kk & 1)) * 1024 + lane * 16, trfrag(lds + L_WOR + (8 * grp + q4) * 64 + i * 32 + p4 * 8, 256));
            }
            VM_WAIT(2);
        } else VM_WAIT(0);
        PIN(g1);
        settle16<true>(g1, wsr, m1 + tid * 16, tag);
        *(f32x2*)(smem + L_MODF + (tid >> 8) * 2048 + (tid >> 1 & 1) * 1024 + (tid >> 2 & 63) * 16 + (tid & 1) * 8) = (f32x2){bflo(g1[0]), bflo(g1[2])};
        LDS_BAR();
        {
            const char* mp = smem + L_MODF + lane * 16;
            const f32x4 ca0 = *(const f32x4*)(mp + 2048), ca1 = *(const f32x4*)(mp + 3072), sa0 = *(const f32x4*)mp, sa1 = *(const f32x4*)(mp + 1024);
            const f32x2 c[4] = {{ca0[0], ca0[1]}, {ca0[2], ca0[3]}, {ca1[0], ca1[1]}, {ca1[2], ca1[3]}}, s[4] = {{sa0[0], sa0[1]}, {sa0[2], sa0[3]}, {sa1[0], sa1[1]}, {sa1[2], sa1[3]}};
            unsigned t[16];
#define FR_RD(k0) do { SBAR(); _Pragma("unroll") for (int k = k0; k < k0 + 2; ++k) { wf[k][0] = trfrag(wp0 + k * 8192, 1024); wf[k][1] = trfrag(wp1 + k * 8192, 1024); } SBAR(); } while (0)
            FR_RD(14);
#pragma unroll
            for (int i = 0; i < 16; ++i) v[i] *= c[i & 3];
            SBAR();
#pragma unroll
            for (int i = 0; i < 16; ++i) t[i] = cvtpk(v[i][0], v[i][1]);
            SBAR();
#pragma unroll
            for (int i = 0; i < 16; ++i) v[i] = bf2(t[i]);
            SBAR();
#pragma unroll
            for (int i = 0; i < 16; ++i) v[i] += s[i & 3];
            SBAR();
            char* hp = smem + L_H + (wid >> 2) * 16384 + (lane >> 2) * 1024 + (lane & 3) * 256;
#pragma unroll
            for (int i = 0; i < 4; ++i)
                *(u32x4*)(hp + ((((wid & 3) * 4 + i) ^ (lane & 7)) * 16)) = (u32x4){cvtpk(v[4 * i][0], v[4 * i][1]), cvtpk(v[4 * i + 1][0], v[4 * i + 1][1]), cvtpk(v[4 * i + 2][0], v[4 * i + 2][1]), cvtpk(v[4 * i + 3][0], v[4 * i + 3][1])};
        }
        LDS_BAR();
        const char* hb0 = smem + L_H + mb * 16384 + (grp * 16 + (l16 ^ grp)) * 16;
        const char* hb1 = smem + L_H + mb * 16384 + (grp * 16 + (l16 ^ (grp | 4))) * 16;
        u32x4 hf[16];
#pragma unroll
        for (int k = 0; k < 16; ++k) hf[k] = *(const u32x4*)(((k & 1) ? hb1 : hb0) + k * 1024);
        f32x4 acc0 = {0.f, 0.f, 0.f, 0.f}, acc1 = acc0;
        if (wid == kv_wave(cg)) {
            const LAS char* wp = lds + L_WKV + (16 * (grp >> 1) + 4 * (grp & 1) + q4) * 32 + p4 * 8;
            f32x4 a0 = acc0;
            u32x4 we[6];
            __builtin_amdgcn_s_setprio(1);
#pragma unroll
            for (int k = 0; k < 6; ++k) we[k] = trfrag(wp + k * 1024, 256);
            SBAR();
            if (cg < 2) {
#pragma unroll
                for (int k = 0; k < 16; ++k) { a0 = mfma16(we[k % 6], hf[k], a0); SBAR(); if (k + 6 < 16) we[k % 6] = trfrag(wp + (k + 6) * 1024, 256); SBAR(); }
                const unsigned w0 = cvtpk(a0[0], a0[1]), w1 = cvtpk(a0[2], a0[3]);
                const auto r0 = __builtin_amdgcn_permlane16_swap(w0, w0, false, false), r1 = __builtin_amdgcn_permlane16_swap(w1, w1, false, false);
                if (!(grp & 1)) st16_sc1(wsr, WS_KT + rt * 1024 + ((grp >> 1) * 32 + mb * 16 + l16) * 16, (u32x4){r0[0], r1[0], r0[1], r1[1]});
            } else {
#pragma unroll
                for (int k = 0; k < 16; ++k) { a0 = mfma16(hf[k], we[k % 6], a0); SBAR(); if (k + 6 < 16) we[k % 6] = trfrag(wp + (k + 6) * 1024, 256); SBAR(); }
                const unsigned w0 = cvtpk(a0[0], a0[1]), w1 = cvtpk(a0[2], a0[3]);
                const auto r0 = __builtin_amdgcn_permlane32_swap(w0, w0, false, false), r1 = __builtin_amdgcn_permlane32_swap(w1, w1, false, false);
                if (lane < 32) st16_sc1(wsr, WS_VT + rt * 1024 + mb * 512 + lane * 16, (u32x4){r0[0], r1[0], r0[1], r1[1]});
            }
            SBAR();
        }
#pragma unroll
        for (int k = 0; k < 16; ++k) { acc0 = mfma16(wf[k][0], hf[k], acc0); acc1 = mfma16(wf[k][1], hf[k], acc1); }
        if (wid == kv_wave(cg)) {
            VM_WAIT(0);
            if (lane < RKV) st8_sc1(wsr, WS_FKV + (lane * 256 + cw) * KVS, tag);
            __builtin_amdgcn_s_setprio(0);
        }
        {
            constexpr float QS = 0.25f * 1.4426950408889634f;
            acc0 *= QS; acc1 *= QS;
            const auto r0 = __builtin_amdgcn_permlane16_swap(cvtpk(acc0[0], acc0[1]), cvtpk(acc1[0], acc1[1]), false, false);
            const auto r1 = __builtin_amdgcn_permlane16_swap(cvtpk(acc0[2], acc0[3]), cvtpk(acc1[2], acc1[3]), false, false);
            const int token = t0 + mb * 16 + l16, head = cg * 8 + n0 + (grp & 1);
            st16_sc1(wsr, WS_Q + (token >> 3) * 8192 + (head >> 2) * 1024 + ((grp >> 1) * 32 + (head & 3) * 8 + (token & 7)) * 16, (u32x4){r0[0], r1[0], r0[1], r1[1]});
        }
        BLOAD1(g2, wsr, tid * 16, WS_MOD, 0);
        BLOAD1(g3, wsr, (tid & 255) * 16, WS_MOD + 8192, 0);
    }
    const int tA = (xg * 32 + idx) * 8, m64 = xg * 4 + (idx >> 3), nt = idx & 7;
    u32x16 wdA, wdB, wdC, wdD, wdE, wdF, wdG;
    bool wd_early;
    {
        int lane = lane_id();
        const int q32 = lane & 31, hf = lane >> 5, wb = wid < 7 ? wid : 6;
        f32x8 xa;
        unsigned lmin, lmax;
        u32x4 xr; u32x16 v2, v3; u64 look;
        {
            const unsigned one = (q32 & 27) == 16 ? 0x3f803f80u : 0u;
            u32x16 v0 = {one, one, one, one, one, one, one, one, one, one, one, one, one, one, one, one}, v1 = v0;
            v2 = v0; v3 = v0;
            const unsigned a_pw = L_PART + wid * 2304 + lane * 4, a_pr = L_PART + wid * 256 + lane * 4, a_pl = L_PART + 2048 + lane * 4;
            const unsigned a_ow = L_OB + (q32 >> 4) * 1024 + ((((q32 >> 3) & 1) * 2 + (wid >> 2)) * 16 + (q32 & 7)) * 16 + ((wid & 3) + 4 * hf) * 2;
            const unsigned a_or = L_OB + ((lane & 48) + (lane & 7)) * 16, l16b = lane * 16, voff = (hf * 16 + (q32 & 15)) * 16;
            const int sl = wb * 64 + lane, row = sl / 14, c = sl - row * 14;
            const char* wsrc = (const char*)((c < 7 ? wg + c * 8 : wu + (c - 7) * 8) + (size_t)row * FF + idx * 56);
            const int fr = b * 7 + wb, kg = (fr >> 1) % 112, sg = 2 * (kg % 7) + (lane >> 5);
            const char* dsrc = (const char*)(wd + (size_t)((kg / 7) * 112 + (sg >= 7 ? 56 : 0) + wd_row(sg >= 7 ? sg - 7 : sg, (lane >> 4) & 1) + ((lane >> 2) & 3)) * DM
                                             + (fr / 224) * 64 + (fr & 1) * 32 + (lane & 3) * 8);
            const unsigned tdst = L_PART + 18432 + wid * 2304;
            const unsigned a_tr = tdst + (hf * 8 + ((lane & 15) >> 2)) * 64 + ((lane >> 4) & 1) * 32 + (lane & 3) * 8, t_off = WS_WD + fr * 1024 + lane * 16, f_off = WS_FWD + fr * 8;
            u64 vtag = tag;
            unsigned p_pw = a_pw, p_pr = a_pr, p_pl = a_pl, p_ow = a_ow, p_or = a_or, p_l = l16b, p_v = voff, p_tr = a_tr, p_t = t_off, p_f = f_off;
            unsigned p_x = (lane & 7) * 1024 + (wid * 64 + (lane & 8) * 4 + (lane >> 4) * 4) * 2, p_m = L_MOD2 + (wid * 64 + (lane & 8) * 4 + (lane >> 4) * 4) * 4;
            look = (u64)dsrc;
            unsigned po = WS_FKV + ((cw / (256 / RKV)) * 256 + wid * 32 + (lane & 31)) * KVS;
            unsigned p_q = WS_F2 + (((cw & ~3) + ((lane >> 2) & 3)) * 8 + (idx & 2) * 2 + (lane & 3)) * QS_;
            asm volatile("s_waitcnt vmcnt(2)" : "+{v[92:107]}"(v0), "+{v[108:123]}"(v1), "+{v[124:139]}"(v2), "+{v[140:155]}"(v3), "+{v228}"(p_pw), "+{v229}"(p_pr), "+{v230}"(p_pl),
                         "+{v231}"(p_ow), "+{v232}"(p_or), "+{v233}"(p_q), "+{v234}"(p_l), "+{v235}"(p_v), "+{v[236:237]}"(wsrc), "+{v[244:245]}"(look), "+{v252}"(p_x), "+{v253}"(p_m), "+{v246}"(p_tr), "+{v247}"(p_t),
                         "+{v248}"(p_f), "+{v[250:251]}"(vtag), "+v"(po) :: "memory");
            if (lane == 0) st8_sc1(wsr, WS_F2 + (cw * 8 + wid) * QS_, tag);
            VM_WAIT(1);
            PIN(g2); PIN(g3);
            const int tid = wid * 64 + lane;
            settle16<true>(g2, wsr, WS_MOD + tid * 16, tag);
            settle16<true>(g3, wsr, WS_MOD + 8192 + (tid & 255) * 16, tag);
            *(f32x2*)(smem + L_MOD2 + tid * 8) = (f32x2){bflo(g2[0]), bflo(g2[2])};
            *(f32x2*)(smem + L_MOD2 + 4096 + (tid & 255) * 8) = (f32x2){bflo(g3[0]), bflo(g3[2])};
            if (tid == 0) *(int*)(smem + L_FLAG) = 0;
            unsigned o_sfwd = WS_FWD + (nt * 112 + wid * 14) * 16;
            unsigned o_swd = WS_WD + (nt * 112 + wid * 14) * 2048;
            unsigned o_tdst = tdst;
            u64 o_upmask = 0xff00ff00ff00ff00ull;
            unsigned o_sk0 = WS_KT + wid * 8192;
            unsigned o_sk4 = WS_KT + wid * 8192 + 4096;
            unsigned o_sv0 = WS_VT + wid * 8192;
            unsigned o_sv4 = WS_VT + wid * 8192 + 4096;
            unsigned o_swo = WS_WO + wid * 8192;
            unsigned o_sq = WS_Q + (tA >> 3) * 8192;
            unsigned o_rot = (idx & 3) * 2;
            u64 o_xb = (u64)(x + (size_t)tA * DM);
            unsigned o_spf = WS_WD + (idx * 7 + wb) * 8192;
            unsigned o_wdst = wb * 1024;
            u64 o_wstep = (u64)(32 * FF * 2);
            u64 o_vmask = 0x0000ffff0000ffffull;
            asm volatile("" : "+s"(o_sfwd), "+s"(o_swd), "+s"(o_tdst), "+s"(o_upmask), "+s"(o_sk0), "+s"(o_sk4), "+s"(o_sv0), "+s"(o_sv4), "+s"(o_swo), "+s"(o_sq), "+s"(o_rot), "+s"(o_xb), "+s"(o_spf), "+s"(o_wdst), "+s"(o_wstep), "+s"(o_vmask));
            LDS_BAR();
            poll8(wsr, po, tag);
            unsigned st, m0s, sa_, sb_;
            asm volatile(
    "s_mov_b32 %[m0s], m0\n"
    "s_add_u32 %[st], %[rot], 0\n"
    "s_and_b32 %[st], %[st], 7\n"
    "s_lshl_b32 %[st], %[st], 10\n"
    "s_add_u32 %[st], %[st], %[sq]\n"
    "buffer_load_dwordx4 v[48:51], v234, %[rs], %[st] offen sc1\n"
    "buffer_load_dwordx4 v[60:63], v234, %[rs], %[sk0] offen offset:0 sc1\n"
    "s_mov_b64 exec, %[vmask]\n"
    "buffer_load_dwordx4 v[92:95], v235, %[rs], %[sv0] offen offset:0 sc1\n"
    "buffer_load_dwordx4 v[96:99], v235, %[rs], %[sv0] offen offset:512 sc1\n"
    "s_mov_b64 exec, -1\n"
    "buffer_load_dwordx4 v[64:67], v234, %[rs], %[sk0] offen offset:1024 sc1\n"
    "s_mov_b64 exec, %[vmask]\n"
    "buffer_load_dwordx4 v[100:103], v235, %[rs], %[sv0] offen offset:1024 sc1\n"
    "buffer_load_dwordx4 v[104:107], v235, %[rs], %[sv0] offen offset:1536 sc1\n"
    "s_mov_b64 exec, -1\n"
    "buffer_load_dwordx4 v[68:71], v234, %[rs], %[sk0] offen offset:2048 sc1\n"
    "s_mov_b64 exec, %[vmask]\n"
    "buffer_load_dwordx4 v[108:111], v235, %[rs], %[sv0] offen offset:2048 sc1\n"
    "buffer_load_dwordx4 v[112:115], v235, %[rs], %[sv0] offen offset:2560 sc1\n"
    "s_mov_b64 exec, -1\n"
    "v_mov_b32 v238, -1\n"
    "v_mov_b32 v239, 0\n"
    "s_waitcnt vmcnt(8)\n"
    "v_mfma_f32_32x32x16_bf16 v[16:31], v[60:63], v[48:51], 0\n"
    "s_nop 7\n"
    "s_nop 7\n"
    "v_exp_f32 v16, v16\n"
    "v_exp_f32 v17, v17\n"
    "v_exp_f32 v18, v18\n"
    "v_exp_f32 v19, v19\n"
    "v_exp_f32 v20, v20\n"
    "v_exp_f32 v21, v21\n"
    "v_cvt_pk_bf16_f32 v52, v16, v17\n"
    "v_exp_f32 v22, v22\n"
    "v_exp_f32 v23, v23\n"
    "v_cvt_pk_bf16_f32 v53, v18, v19\n"
    "buffer_load_dwordx4 v[72:75], v234, %[rs], %[sk0] offen offset:3072 sc1\n"
    "s_waitcnt vmcnt(6)\n"
    "v_mfma_f32_32x32x16_bf16 v[32:47], v[64:67], v[48:51], 0\n"
    "v_exp_f32 v24, v24\n"
    "v_exp_f32 v25, v25\n"
    "v_cvt_pk_bf16_f32 v54, v20, v21\n"
    "v_exp_f32 v26, v26\n"
    "v_exp_f32 v27, v27\n"
    "v_cvt_pk_bf16_f32 v55, v22, v23\n"
    "s_mov_b64 exec, %[vmask]\n"
    "buffer_load_dwordx4 v[116:119], v235, %[rs], %[sv0] offen offset:3072 sc1\n"
    "buffer_load_dwordx4 v[120:123], v235, %[rs], %[sv0] offen offset:3584 sc1\n"
    "s_mov_b64 exec, -1\n"
    "v_exp_f32 v28, v28\n"
    "v_exp_f32 v29, v29\n"
    "v_mfma_f32_32x32x16_bf16 v[0:15], v[92:95], v[52:55], 0\n"
    "v_cvt_pk_bf16_f32 v56, v24, v25\n"
    "v_exp_f32 v30, v30\n"
    "v_exp_f32 v31, v31\n"
    "v_cvt_pk_bf16_f32 v57, v26, v27\n"
    "v_exp_f32 v32, v32\n"
    "v_exp_f32 v33, v33\n"
    "v_cvt_pk_bf16_f32 v58, v28, v29\n"
    "v_exp_f32 v34, v34\n"
    "v_exp_f32 v35, v35\n"
    "v_cvt_pk_bf16_f32 v59, v30, v31\n"
    "v_exp_f32 v36, v36\n"
    "v_exp_f32 v37, v37\n"
    "v_mfma_f32_32x32x16_bf16 v[0:15], v[96:99], v[56:59], v[0:15]\n"
    "v_cvt_pk_bf16_f32 v52, v32, v33\n"
    "v_exp_f32 v38, v38\n"
    "v_exp_f32 v39, v39\n"
    "v_cvt_pk_bf16_f32 v53, v34, v35\n"
    "buffer_load_dwordx4 v[76:79], v234, %[rs], %[sk4] offen offset:0 sc1\n"
    "s_waitcnt vmcnt(6)\n"
    "v_mfma_f32_32x32x16_bf16 v[16:31], v[68:71], v[48:51], 0\n"
    "v_exp_f32 v40, v40\n"
    "v_exp_f32 v41, v41\n"
    "v_cvt_pk_bf16_f32 v54, v36, v37\n"
    "v_exp_f32 v42, v42\n"
    "v_exp_f32 v43, v43\n"
    "v_cvt_pk_bf16_f32 v55, v38, v39\n"
    "s_mov_b64 exec, %[vmask]\n"
    "buffer_load_dwordx4 v[124:127], v235, %[rs], %[sv4] offen offset:0 sc1\n"
    "buffer_load_dwordx4 v[128:131], v235, %[rs], %[sv4] offen offset:512 sc1\n"
    "s_mov_b64 exec, -1\n"
    "v_exp_f32 v44, v44\n"
    "v_exp_f32 v45, v45\n"
    "v_mfma_f32_32x32x16_bf16 v[0:15], v[100:103], v[52:55], v[0:15]\n"
    "v_cvt_pk_bf16_f32 v56, v40, v41\n"
    "v_exp_f32 v46, v46\n"
    "v_exp_f32 v47, v47\n"
    "v_cvt_pk_bf16_f32 v57, v42, v43\n"
    "v_exp_f32 v16, v16\n"
    "v_exp_f32 v17, v17\n"
    "v_cvt_pk_bf16_f32 v58, v44, v45\n"
    "v_exp_f32 v18, v18\n"
    "v_exp_f32 v19, v19\n"
    "v_cvt_pk_bf16_f32 v59, v46, v47\n"
    "v_exp_f32 v20, v20\n"
    "v_exp_f32 v21, v21\n"
    "v_mfma_f32_32x32x16_bf16 v[0:15], v[104:107], v[56:59], v[0:15]\n"
    "v_cvt_pk_bf16_f32 v52, v16, v17\n"
    "v_exp_f32 v22, v22\n"
    "v_exp_f32 v23, v23\n"
    "v_cvt_pk_bf16_f32 v53, v18, v19\n"
    "buffer_load_dwordx4 v[80:83], v234, %[rs], %[sk4] offen offset:1024 sc1\n"
    "s_waitcnt vmcnt(6)\n"
    "v_mfma_f32_32x32x16_bf16 v[32:47], v[72:75], v[48:51], 0\n"
    "v_exp_f32 v24, v24\n"
    "v_exp_f32 v25, v25\n"
    "v_cvt_pk_bf16_f32 v54, v20, v21\n"
    "v_exp_f32 v26, v26\n"
    "v_exp_f32 v27, v27\n"
    "v_cvt_pk_bf16_f32 v55, v22, v23\n"
    "s_mov_b64 exec, %[vmask]\n"
    "buffer_load_dwordx4 v[132:135], v235, %[rs], %[sv4] offen offset:1024 sc1\n"
    "buffer_load_dwordx4 v[136:139], v235, %[rs], %[sv4] offen offset:1536 sc1\n"
    "s_mov_b64 exec, -1\n"
    "v_exp_f32 v28, v28\n"
    "v_exp_f32 v29, v29\n"
    "v_mfma_f32_32x32x16_bf16 v[0:15], v[108:111], v[52:55], v[0:15]\n"
    "v_cvt_pk_bf16_f32 v56, v24, v25\n"
    "v_exp_f32 v30, v30\n"
    "v_exp_f32 v31, v31\n"
    "v_cvt_pk_bf16_f32 v57, v26, v27\n"
    "v_exp_f32 v32, v32\n"
    "v_exp_f32 v33, v33\n"
    "v_cvt_pk_bf16_f32 v58, v28, v29\n"
    "v_exp_f32 v34, v34\n"
    "v_exp_f32 v35, v35\n"
    "v_cvt_pk_bf16_f32 v59, v30, v31\n"
    "v_exp_f32 v36, v36\n"
    "v_exp_f32 v37, v37\n"
    "v_mfma_f32_32x32x16_bf16 v[0:15], v[112:115], v[56:59], v[0:15]\n"
    "v_cvt_pk_bf16_f32 v52, v32, v33\n"
    "v_exp_f32 v38, v38\n"
    "v_exp_f32 v39, v39\n"
    "v_cvt_pk_bf16_f32 v53, v34, v35\n"
    "buffer_load_dwordx4 v[84:87], v234, %[rs], %[sk4] offen offset:2048 sc1\n"
    "s_waitcnt vmcnt(6)\n"
    "v_mfma_f32_32x32x16_bf16 v[16:31], v[76:79], v[48:51], 0\n"
    "v_exp_f32 v40, v40\n"
    "v_exp_f32 v41, v41\n"
    "v_cvt_pk_bf16_f32 v54, v36, v37\n"
    "v_exp_f32 v42, v42\n"
    "v_exp_f32 v43, v43\n"
    "v_cvt_pk_bf16_f32 v55, v38, v39\n"
    "s_mov_b64 exec, %[vmask]\n"
    "buffer_load_dwordx4 v[140:143], v235, %[rs], %[sv4] offen offset:2048 sc1\n"
    "buffer_load_dwordx4 v[144:147], v235, %[rs], %[sv4] offen offset:2560 sc1\n"
    "s_mov_b64 exec, -1\n"
    "v_exp_f32 v44, v44\n"
    "v_exp_f32 v45, v45\n"
    "v_mfma_f32_32x32x16_bf16 v[0:15], v[116:119], v[52:55], v[0:15]\n"
    "v_cvt_pk_bf16_f32 v56, v40, v41\n"
    "v_exp_f32 v46, v46\n"
    "v_exp_f32 v47, v47\n"
    "v_cvt_pk_bf16_f32 v57, v42, v43\n"
    "v_exp_f32 v16, v16\n"
    "v_exp_f32 v17, v17\n"
    "v_cvt_pk_bf16_f32 v58, v44, v45\n"
    "v_exp_f32 v18, v18\n"
    "v_exp_f32 v19, v19\n"
    "v_cvt_pk_bf16_f32 v59, v46, v47\n"
    "v_exp_f32 v20, v20\n"
    "v_exp_f32 v21, v21\n"
    "v_mfma_f32_32x32x16_bf16 v[0:15], v[120:123], v[56:59], v[0:15]\n"
    "v_cvt_pk_bf16_f32 v52, v16, v17\n"
    "v_exp_f32 v22, v22\n"
    "v_exp_f32 v23, v23\n"
    "v_cvt_pk_bf16_f32 v53, v18, v19\n"
    "buffer_load_dwordx4 v[88:91], v234, %[rs], %[sk4] offen offset:3072 sc1\n"
    "s_waitcnt vmcnt(6)\n"
    "v_mfma_f32_32x32x16_bf16 v[32:47], v[80:83], v[48:51], 0\n"
    "v_exp_f32 v24, v24\n"
    "v_exp_f32 v25, v25\n"
    "v_cvt_pk_bf16_f32 v54, v20, v21\n"
    "v_exp_f32 v26, v26\n"
    "v_exp_f32 v27, v27\n"
    "v_cvt_pk_bf16_f32 v55, v22, v23\n"
    "s_mov_b64 exec, %[vmask]\n"
    "buffer_load_dwordx4 v[148:151], v235, %[rs], %[sv4] offen offset:3072 sc1\n"
    "buffer_load_dwordx4 v[152:155], v235, %[rs], %[sv4] offen offset:3584 sc1\n"
    "s_mov_b64 exec, -1\n"
    "v_exp_f32 v28, v28\n"
    "v_exp_f32 v29, v29\n"
    "v_mfma_f32_32x32x16_bf16 v[0:15], v[124:127], v[52:55], v[0:15]\n"
    "v_cvt_pk_bf16_f32 v56, v24, v25\n"
    "v_exp_f32 v30, v30\n"
    "v_exp_f32 v31, v31\n"
    "v_cvt_pk_bf16_f32 v57, v26, v27\n"
    "v_exp_f32 v32, v32\n"
    "v_exp_f32 v33, v33\n"
    "v_cvt_pk_bf16_f32 v58, v28, v29\n"
    "v_exp_f32 v34, v34\n"
    "v_exp_f32 v35, v35\n"
    "v_cvt_pk_bf16_f32 v59, v30, v31\n"
    "v_exp_f32 v36, v36\n"
    "v_exp_f32 v37, v37\n"
    "v_mfma_f32_32x32x16_bf16 v[0:15], v[128:131], v[56:59], v[0:15]\n"
    "v_cvt_pk_bf16_f32 v52, v32, v33\n"
    "v_exp_f32 v38, v38\n"
    "v_exp_f32 v39, v39\n"
    "v_cvt_pk_bf16_f32 v53, v34, v35\n"
    "s_waitcnt vmcnt(5)\n"
    "v_mfma_f32_32x32x16_bf16 v[16:31], v[84:87], v[48:51], 0\n"
    "v_exp_f32 v40, v40\n"
    "v_exp_f32 v41, v41\n"
    "s_add_u32 %[st], %[rot], 1\n"
    "s_and_b32 %[st], %[st], 7\n"
    "s_lshl_b32 %[st], %[st], 10\n"
    "s_add_u32 %[st], %[st], %[sq]\n"
    "buffer_load_dwordx4 v[240:243], v234, %[rs], %[st] offen sc1\n"
    "v_cvt_pk_bf16_f32 v54, v36, v37\n"
    "v_exp_f32 v42, v42\n"
    "v_exp_f32 v43, v43\n"
    "v_cvt_pk_bf16_f32 v55, v38, v39\n"
    "v_exp_f32 v44, v44\n"
    "v_exp_f32 v45, v45\n"
    "v_mfma_f32_32x32x16_bf16 v[0:15], v[132:135], v[52:55], v[0:15]\n"
    "v_cvt_pk_bf16_f32 v56, v40, v41\n"
    "v_exp_f32 v46, v46\n"
    "v_exp_f32 v47, v47\n"
    "v_cvt_pk_bf16_f32 v57, v42, v43\n"
    "v_exp_f32 v16, v16\n"
    "v_exp_f32 v17, v17\n"
    "v_cvt_pk_bf16_f32 v58, v44, v45\n"
    "v_exp_f32 v18, v18\n"
    "v_exp_f32 v19, v19\n"
    "s_mov_b32 m0, %[tdst]\n"
    "s_nop 0\n"
    "global_load_lds_dwordx4 v[244:245], off\n"
    "v_cvt_pk_bf16_f32 v59, v46, v47\n"
    "v_exp_f32 v20, v20\n"
    "v_exp_f32 v21, v21\n"
    "v_mfma_f32_32x32x16_bf16 v[0:15], v[136:139], v[56:59], v[0:15]\n"
    "v_cvt_pk_bf16_f32 v52, v16, v17\n"
    "v_exp_f32 v22, v22\n"
    "v_exp_f32 v23, v23\n"
    "v_cvt_pk_bf16_f32 v53, v18, v19\n"
    "s_waitcnt vmcnt(4)\n"
    "v_mfma_f32_32x32x16_bf16 v[32:47], v[88:91], v[48:51], 0\n"
    "v_exp_f32 v24, v24\n"
    "v_exp_f32 v25, v25\n"
    "v_cvt_pk_bf16_f32 v54, v20, v21\n"
    "v_exp_f32 v26, v26\n"
    "v_exp_f32 v27, v27\n"
    "v_cvt_pk_bf16_f32 v55, v22, v23\n"
    "v_exp_f32 v28, v28\n"
    "v_exp_f32 v29, v29\n"
    "v_mfma_f32_32x32x16_bf16 v[0:15], v[140:143], v[52:55], v[0:15]\n"
    "v_cvt_pk_bf16_f32 v56, v24, v25\n"
    "v_exp_f32 v30, v30\n"
    "v_exp_f32 v31, v31\n"
    "v_cvt_pk_bf16_f32 v57, v26, v27\n"
    "v_exp_f32 v32, v32\n"
    "v_exp_f32 v33, v33\n"
    "v_cvt_pk_bf16_f32 v58, v28, v29\n"
    "v_exp_f32 v34, v34\n"
    "v_exp_f32 v35, v35\n"
    "v_cvt_pk_bf16_f32 v59, v30, v31\n"
    "v_exp_f32 v36, v36\n"
    "v_exp_f32 v37, v37\n"
    "v_mfma_f32_32x32x16_bf16 v[0:15], v[144:147], v[56:59], v[0:15]\n"
    "v_cvt_pk_bf16_f32 v52, v32, v33\n"
    "v_exp_f32 v38, v38\n"
    "v_exp_f32 v39, v39\n"
    "v_cvt_pk_bf16_f32 v53, v34, v35\n"
    "s_waitcnt vmcnt(1)\n"
    "v_mfma_f32_32x32x16_bf16 v[16:31], v[60:63], v[240:243], 0\n"
    "v_exp_f32 v40, v40\n"
    "v_exp_f32 v41, v41\n"
    "buffer_load_dwordx2 v[244:245], v233, %[rs], 0 offen sc1\n"
    "v_cvt_pk_bf16_f32 v54, v36, v37\n"
    "v_exp_f32 v42, v42\n"
    "v_exp_f32 v43, v43\n"
    "v_cvt_pk_bf16_f32 v55, v38, v39\n"
    "v_exp_f32 v44, v44\n"
    "v_exp_f32 v45, v45\n"
    "v_mfma_f32_32x32x16_bf16 v[0:15], v[148:151], v[52:55], v[0:15]\n"
    "v_cvt_pk_bf16_f32 v56, v40, v41\n"
    "v_exp_f32 v46, v46\n"
    "v_exp_f32 v47, v47\n"
    "v_cvt_pk_bf16_f32 v57, v42, v43\n"
    "v_exp_f32 v16, v16\n"
    "v_exp_f32 v17, v17\n"
    "v_cvt_pk_bf16_f32 v58, v44, v45\n"
    "v_exp_f32 v18, v18\n"
    "v_exp_f32 v19, v19\n"
    "v_cvt_pk_bf16_f32 v59, v46, v47\n"
    "v_exp_f32 v20, v20\n"
    "v_exp_f32 v21, v21\n"
    "v_mfma_f32_32x32x16_bf16 v[0:15], v[152:155], v[56:59], v[0:15]\n"
    "v_cvt_pk_bf16_f32 v52, v16, v17\n"
    "v_exp_f32 v22, v22\n"
    "v_exp_f32 v23, v23\n"
    "v_cvt_pk_bf16_f32 v53, v18, v19\n"
    "v_mfma_f32_32x32x16_bf16 v[32:47], v[64:67], v[240:243], 0\n"
    "v_exp_f32 v24, v24\n"
    "v_exp_f32 v25, v25\n"
    "s_add_u32 m0, %[wdst], 0\n"
    "s_nop 0\n"
    "global_load_lds_dwordx4 v[236:237], off\n"
    "v_cvt_pk_bf16_f32 v54, v20, v21\n"
    "v_exp_f32 v26, v26\n"
    "v_exp_f32 v27, v27\n"
    "v_cvt_pk_bf16_f32 v55, v22, v23\n"
    "v_exp_f32 v28, v28\n"
    "v_exp_f32 v29, v29\n"
    "v_mfma_f32_32x32x16_bf16 v[212:227], v[92:95], v[52:55], 0\n"
    "v_cvt_pk_bf16_f32 v56, v24, v25\n"
    "v_exp_f32 v30, v30\n"
    "ds_write2st64_b32 v228, v0, v1 offset0:0 offset1:1\n"
    "v_exp_f32 v31, v31\n"
    "v_cvt_pk_bf16_f32 v57, v26, v27\n"
    "v_exp_f32 v32, v32\n"
    "ds_write2st64_b32 v228, v2, v3 offset0:2 offset1:3\n"
    "v_exp_f32 v33, v33\n"
    "v_cvt_pk_bf16_f32 v58, v28, v29\n"
    "v_exp_f32 v34, v34\n"
    "ds_write2st64_b32 v228, v4, v5 offset0:4 offset1:5\n"
    "v_exp_f32 v35, v35\n"
    "v_cvt_pk_bf16_f32 v59, v30, v31\n"
    "v_exp_f32 v36, v36\n"
    "ds_write2st64_b32 v228, v6, v7 offset0:6 offset1:7\n"
    "v_exp_f32 v37, v37\n"
    "v_mfma_f32_32x32x16_bf16 v[212:227], v[96:99], v[56:59], v[212:227]\n"
    "v_cvt_pk_bf16_f32 v52, v32, v33\n"
    "v_exp_f32 v38, v38\n"
    "ds_write_b32 v228, v8 offset:2048\n"
    "v_exp_f32 v39, v39\n"
    "v_cvt_pk_bf16_f32 v53, v34, v35\n"
    "v_mfma_f32_32x32x16_bf16 v[16:31], v[68:71], v[240:243], 0\n"
    "v_exp_f32 v40, v40\n"
    "v_exp_f32 v41, v41\n"
    "v_cvt_pk_bf16_f32 v54, v36, v37\n"
    "v_exp_f32 v42, v42\n"
    "v_exp_f32 v43, v43\n"
    "v_cvt_pk_bf16_f32 v55, v38, v39\n"
    "v_exp_f32 v44, v44\n"
    "v_exp_f32 v45, v45\n"
    "v_mfma_f32_32x32x16_bf16 v[212:227], v[100:103], v[52:55], v[212:227]\n"
    "v_cvt_pk_bf16_f32 v56, v40, v41\n"
    "v_exp_f32 v46, v46\n"
    "v_exp_f32 v47, v47\n"
    "v_cvt_pk_bf16_f32 v57, v42, v43\n"
    "s_waitcnt lgkmcnt(0)\n"
    "s_barrier\n"
    "v_exp_f32 v16, v16\n"
    "ds_read2st64_b32 v[8:9], v230 offset0:0 offset1:9\n"
    "v_exp_f32 v17, v17\n"
    "v_cvt_pk_bf16_f32 v58, v44, v45\n"
    "v_exp_f32 v18, v18\n"
    "ds_read2st64_b32 v[10:11], v230 offset0:18 offset1:27\n"
    "v_exp_f32 v19, v19\n"
    "s_add_u32 %[sa], %[rot], 0\n"
    "s_and_b32 %[sa], %[sa], 7\n"
    "s_lshl_b32 %[sa], %[sa], 16\n"
    "s_add_u32 %[sa], %[sa], %[swo]\n"
    "buffer_load_dwordx4 v[156:159], v234, %[rs], %[sa] offen offset:0 sc1\n"
    "v_cvt_pk_bf16_f32 v59, v46, v47\n"
    "v_exp_f32 v20, v20\n"
    "ds_read2st64_b32 v[12:13], v230 offset0:36 offset1:45\n"
    "v_exp_f32 v21, v21\n"
    "v_mfma_f32_32x32x16_bf16 v[212:227], v[104:107], v[56:59], v[212:227]\n"
    "v_cvt_pk_bf16_f32 v52, v16, v17\n"
    "v_exp_f32 v22, v22\n"
    "ds_read2st64_b32 v[14:15], v230 offset0:54 offset1:63\n"
    "v_exp_f32 v23, v23\n"
    "v_cvt_pk_bf16_f32 v53, v18, v19\n"
    "v_mfma_f32_32x32x16_bf16 v[32:47], v[72:75], v[240:243], 0\n"
    "v_exp_f32 v24, v24\n"
    "ds_read2st64_b32 v[0:1], v229 offset0:0 offset1:9\n"
    "v_exp_f32 v25, v25\n"
    "s_waitcnt vmcnt(2)\n"
    "s_mov_b32 %[st], 0x20000\n"
    ".Lq0:\n"
    "v_cmp_ne_u64 vcc, %[tag], v[244:245]\n"
    "s_nop 0\n"
    "s_cbranch_vccz .Lq1\n"
    "s_sleep 2\n"
    "buffer_load_dwordx2 v[244:245], v233, %[rs], 0 offen sc1\n"
    "s_sub_u32 %[st], %[st], 1\n"
    "s_cmp_lg_u32 %[st], 0\n"
    "s_waitcnt vmcnt(0)\n"
    "s_cbranch_scc1 .Lq0\n"
    ".Lq1:\n"
    "s_add_u32 %[st], %[rot], 2\n"
    "s_and_b32 %[st], %[st], 7\n"
    "s_lshl_b32 %[st], %[st], 10\n"
    "s_add_u32 %[st], %[st], %[sq]\n"
    "buffer_load_dwordx4 v[48:51], v234, %[rs], %[st] offen sc1\n"
    "v_cvt_pk_bf16_f32 v54, v20, v21\n"
    "v_exp_f32 v26, v26\n"
    "ds_read2st64_b32 v[2:3], v229 offset0:18 offset1:27\n"
    "v_exp_f32 v27, v27\n"
    "v_cvt_pk_bf16_f32 v55, v22, v23\n"
    "v_exp_f32 v28, v28\n"
    "ds_read2st64_b32 v[4:5], v229 offset0:36 offset1:45\n"
    "v_exp_f32 v29, v29\n"
    "v_mfma_f32_32x32x16_bf16 v[212:227], v[108:111], v[52:55], v[212:227]\n"
    "v_cvt_pk_bf16_f32 v56, v24, v25\n"
    "v_exp_f32 v30, v30\n"
    "ds_read2st64_b32 v[6:7], v229 offset0:54 offset1:63\n"
    "v_exp_f32 v31, v31\n"
    "buffer_load_dwordx4 v[164:167], v234, %[rs], %[sa] offen offset:2048 sc1\n"
    "v_cvt_pk_bf16_f32 v57, v26, v27\n"
    "v_exp_f32 v32, v32\n"
    "v_exp_f32 v33, v33\n"
    "s_waitcnt lgkmcnt(4)\n"
    "v_add_f32 v8, v8, v9\n"
    "v_cvt_pk_bf16_f32 v58, v28, v29\n"
    "v_exp_f32 v34, v34\n"
    "v_exp_f32 v35, v35\n"
    "v_add_f32 v10, v10, v11\n"
    "s_add_u32 %[sb], %[rot], 0\n"
    "s_and_b32 %[sb], %[sb], 7\n"
    "s_lshl_b32 %[sb], %[sb], 16\n"
    "s_add_u32 %[sb], %[sb], %[swo]\n"
    "s_add_u32 %[sb], %[sb], 4096\n"
    "buffer_load_dwordx4 v[172:175], v234, %[rs], %[sb] offen offset:0 sc1\n"
    "v_cvt_pk_bf16_f32 v59, v30, v31\n"
    "v_exp_f32 v36, v36\n"
    "v_exp_f32 v37, v37\n"
    "v_add_f32 v12, v12, v13\n"
    "v_mfma_f32_32x32x16_bf16 v[212:227], v[112:115], v[56:59], v[212:227]\n"
    "v_cvt_pk_bf16_f32 v52, v32, v33\n"
    "v_exp_f32 v38, v38\n"
    "v_exp_f32 v39, v39\n"
    "v_add_f32 v14, v14, v15\n"
    "v_cvt_pk_bf16_f32 v53, v34, v35\n"
    "v_mfma_f32_32x32x16_bf16 v[16:31], v[76:79], v[240:243], 0\n"
    "v_exp_f32 v40, v40\n"
    "v_exp_f32 v41, v41\n"
    "v_add_f32 v8, v8, v10\n"
    "v_cvt_pk_bf16_f32 v54, v36, v37\n"
    "v_exp_f32 v42, v42\n"
    "v_exp_f32 v43, v43\n"
    "v_add_f32 v12, v12, v14\n"
    "v_cvt_pk_bf16_f32 v55, v38, v39\n"
    "v_exp_f32 v44, v44\n"
    "v_exp_f32 v45, v45\n"
    "s_waitcnt lgkmcnt(0)\n"
    "v_add_f32 v0, v0, v1\n"
    "v_mfma_f32_32x32x16_bf16 v[212:227], v[116:119], v[52:55], v[212:227]\n"
    "v_cvt_pk_bf16_f32 v56, v40, v41\n"
    "v_exp_f32 v46, v46\n"
    "v_exp_f32 v47, v47\n"
    "v_cvt_pk_bf16_f32 v57, v42, v43\n"
    "v_exp_f32 v16, v16\n"
    "v_exp_f32 v17, v17\n"
    "v_add_f32 v8, v8, v12\n"
    "v_cvt_pk_bf16_f32 v58, v44, v45\n"
    "v_exp_f32 v18, v18\n"
    "v_exp_f32 v19, v19\n"
    "v_add_f32 v2, v2, v3\n"
    "v_cvt_pk_bf16_f32 v59, v46, v47\n"
    "v_exp_f32 v20, v20\n"
    "v_exp_f32 v21, v21\n"
    "v_rcp_f32 v9, v8\n"
    "v_mfma_f32_32x32x16_bf16 v[212:227], v[120:123], v[56:59], v[212:227]\n"
    "v_cvt_pk_bf16_f32 v52, v16, v17\n"
    "v_exp_f32 v22, v22\n"
    "v_exp_f32 v23, v23\n"
    "v_add_f32 v4, v4, v5\n"
    "v_cvt_pk_bf16_f32 v53, v18, v19\n"
    "v_mfma_f32_32x32x16_bf16 v[32:47], v[80:83], v[240:243], 0\n"
    "v_exp_f32 v24, v24\n"
    "v_exp_f32 v25, v25\n"
    "v_add_f32 v6, v6, v7\n"
    "s_add_u32 m0, %[wdst], 7168\n"
    "v_lshl_add_u64 v[236:237], v[236:237], 0, %[wstep]\n"
    "global_load_lds_dwordx4 v[236:237], off\n"
    "v_cvt_pk_bf16_f32 v54, v20, v21\n"
    "v_exp_f32 v26, v26\n"
    "v_exp_f32 v27, v27\n"
    "v_add_f32 v0, v0, v2\n"
    "v_cvt_pk_bf16_f32 v55, v22, v23\n"
    "v_exp_f32 v28, v28\n"
    "v_exp_f32 v29, v29\n"
    "v_add_f32 v4, v4, v6\n"
    "v_mfma_f32_32x32x16_bf16 v[212:227], v[124:127], v[52:55], v[212:227]\n"
    "v_cvt_pk_bf16_f32 v56, v24, v25\n"
    "v_exp_f32 v30, v30\n"
    "v_exp_f32 v31, v31\n"
    "buffer_load_dwordx4 v[180:183], v234, %[rs], %[sb] offen offset:2048 sc1\n"
    "v_cvt_pk_bf16_f32 v57, v26, v27\n"
    "v_exp_f32 v32, v32\n"
    "v_exp_f32 v33, v33\n"
    "v_min_u32 v238, v238, v8\n"
    "v_cvt_pk_bf16_f32 v58, v28, v29\n"
    "v_exp_f32 v34, v34\n"
    "v_exp_f32 v35, v35\n"
    "v_add_f32 v0, v0, v4\n"
    "buffer_load_dwordx4 v[160:163], v234, %[rs], %[sa] offen offset:1024 sc1\n"
    "v_cvt_pk_bf16_f32 v59, v30, v31\n"
    "v_exp_f32 v36, v36\n"
    "v_exp_f32 v37, v37\n"
    "v_add_f32 v8, |v0|, v8\n"
    "v_mfma_f32_32x32x16_bf16 v[212:227], v[128:131], v[56:59], v[212:227]\n"
    "v_cvt_pk_bf16_f32 v52, v32, v33\n"
    "v_exp_f32 v38, v38\n"
    "v_exp_f32 v39, v39\n"
    "v_mul_f32 v0, v0, v9\n"
    "v_cvt_pk_bf16_f32 v53, v34, v35\n"
    "v_mfma_f32_32x32x16_bf16 v[16:31], v[84:87], v[240:243], 0\n"
    "v_exp_f32 v40, v40\n"
    "v_exp_f32 v41, v41\n"
    "v_max_u32 v239, v239, v8\n"
    "ds_read_b64_tr_b16 v[188:189], v246\n"
    "ds_read_b64_tr_b16 v[190:191], v246 offset:256\n"
    "v_cvt_pk_bf16_f32 v54, v36, v37\n"
    "v_exp_f32 v42, v42\n"
    "v_exp_f32 v43, v43\n"
    "v_cvt_pk_bf16_f32 v0, v0, v0\n"
    "v_cvt_pk_bf16_f32 v55, v38, v39\n"
    "v_exp_f32 v44, v44\n"
    "v_exp_f32 v45, v45\n"
    "ds_write_b16 v231, v0 offset:0\n"
    "v_mfma_f32_32x32x16_bf16 v[212:227], v[132:135], v[52:55], v[212:227]\n"
    "v_cvt_pk_bf16_f32 v56, v40, v41\n"
    "v_exp_f32 v46, v46\n"
    "v_exp_f32 v47, v47\n"
    "buffer_load_dwordx4 v[168:171], v234, %[rs], %[sa] offen offset:3072 sc1\n"
    "v_cvt_pk_bf16_f32 v57, v42, v43\n"
    "v_exp_f32 v16, v16\n"
    "v_exp_f32 v17, v17\n"
    "s_waitcnt lgkmcnt(1)\n"
    "buffer_store_dwordx4 v[188:191], v247, %[rs], 0 offen sc1\n"
    "v_cvt_pk_bf16_f32 v58, v44, v45\n"
    "v_exp_f32 v18, v18\n"
    "v_exp_f32 v19, v19\n"
    "buffer_load_dwordx4 v[176:179], v234, %[rs], %[sb] offen offset:1024 sc1\n"
    "v_cvt_pk_bf16_f32 v59, v46, v47\n"
    "v_exp_f32 v20, v20\n"
    "v_exp_f32 v21, v21\n"
    "v_mfma_f32_32x32x16_bf16 v[212:227], v[136:139], v[56:59], v[212:227]\n"
    "v_cvt_pk_bf16_f32 v52, v16, v17\n"
    "v_exp_f32 v22, v22\n"
    "v_exp_f32 v23, v23\n"
    "v_cvt_pk_bf16_f32 v53, v18, v19\n"
    "v_mfma_f32_32x32x16_bf16 v[32:47], v[88:91], v[240:243], 0\n"
    "v_exp_f32 v24, v24\n"
    "v_exp_f32 v25, v25\n"
    "s_add_u32 m0, %[wdst], 14336\n"
    "v_lshl_add_u64 v[236:237], v[236:237], 0, %[wstep]\n"
    "global_load_lds_dwordx4 v[236:237], off\n"
    "v_cvt_pk_bf16_f32 v54, v20, v21\n"
    "v_exp_f32 v26, v26\n"
    "v_exp_f32 v27, v27\n"
    "v_cvt_pk_bf16_f32 v55, v22, v23\n"
    "v_exp_f32 v28, v28\n"
    "v_exp_f32 v29, v29\n"
    "v_mfma_f32_32x32x16_bf16 v[212:227], v[140:143], v[52:55], v[212:227]\n"
    "v_cvt_pk_bf16_f32 v56, v24, v25\n"
    "v_exp_f32 v30, v30\n"
    "v_exp_f32 v31, v31\n"
    "v_cvt_pk_bf16_f32 v57, v26, v27\n"
    "v_exp_f32 v32, v32\n"
    "v_exp_f32 v33, v33\n"
    "v_cvt_pk_bf16_f32 v58, v28, v29\n"
    "v_exp_f32 v34, v34\n"
    "v_exp_f32 v35, v35\n"
    "v_cvt_pk_bf16_f32 v59, v30, v31\n"
    "v_exp_f32 v36, v36\n"
    "v_exp_f32 v37, v37\n"
    "v_mfma_f32_32x32x16_bf16 v[212:227], v[144:147], v[56:59], v[212:227]\n"
    "v_cvt_pk_bf16_f32 v52, v32, v33\n"
    "v_exp_f32 v38, v38\n"
    "v_exp_f32 v39, v39\n"
    "v_cvt_pk_bf16_f32 v53, v34, v35\n"
    "s_waitcnt vmcnt(9)\n"
    "v_mfma_f32_32x32x16_bf16 v[16:31], v[60:63], v[48:51], 0\n"
    "v_exp_f32 v40, v40\n"
    "v_exp_f32 v41, v41\n"
    "v_cvt_pk_bf16_f32 v54, v36, v37\n"
    "v_exp_f32 v42, v42\n"
    "v_exp_f32 v43, v43\n"
    "v_cvt_pk_bf16_f32 v55, v38, v39\n"
    "v_exp_f32 v44, v44\n"
    "v_exp_f32 v45, v45\n"
    "v_mfma_f32_32x32x16_bf16 v[212:227], v[148:151], v[52:55], v[212:227]\n"
    "v_cvt_pk_bf16_f32 v56, v40, v41\n"
    "v_exp_f32 v46, v46\n"
    "v_exp_f32 v47, v47\n"
    "buffer_load_dwordx4 v[184:187], v234, %[rs], %[sb] offen offset:3072 sc1\n"
    "v_cvt_pk_bf16_f32 v57, v42, v43\n"
    "v_exp_f32 v16, v16\n"
    "v_exp_f32 v17, v17\n"
    "v_cvt_pk_bf16_f32 v58, v44, v45\n"
    "v_exp_f32 v18, v18\n"
    "v_exp_f32 v19, v19\n"
    "v_cvt_pk_bf16_f32 v59, v46, v47\n"
    "v_exp_f32 v20, v20\n"
    "v_exp_f32 v21, v21\n"
    "v_mfma_f32_32x32x16_bf16 v[212:227], v[152:155], v[56:59], v[212:227]\n"
    "v_cvt_pk_bf16_f32 v52, v16, v17\n"
    "v_exp_f32 v22, v22\n"
    "v_exp_f32 v23, v23\n"
    "v_cvt_pk_bf16_f32 v53, v18, v19\n"
    "v_mfma_f32_32x32x16_bf16 v[32:47], v[64:67], v[48:51], 0\n"
    "v_exp_f32 v24, v24\n"
    "v_exp_f32 v25, v25\n"
    "s_add_u32 m0, %[wdst], 21504\n"
    "v_lshl_add_u64 v[236:237], v[236:237], 0, %[wstep]\n"
    "global_load_lds_dwordx4 v[236:237], off\n"
    "v_cvt_pk_bf16_f32 v54, v20, v21\n"
    "v_exp_f32 v26, v26\n"
    "v_exp_f32 v27, v27\n"
    "v_cvt_pk_bf16_f32 v55, v22, v23\n"
    "v_exp_f32 v28, v28\n"
    "v_exp_f32 v29, v29\n"
    "v_mfma_f32_32x32x16_bf16 v[0:15], v[92:95], v[52:55], 0\n"
    "v_cvt_pk_bf16_f32 v56, v24, v25\n"
    "v_exp_f32 v30, v30\n"
    "ds_write2st64_b32 v228, v212, v213 offset0:72 offset1:73\n"
    "v_exp_f32 v31, v31\n"
    "v_cvt_pk_bf16_f32 v57, v26, v27\n"
    "v_exp_f32 v32, v32\n"
    "ds_write2st64_b32 v228, v214, v215 offset0:74 offset1:75\n"
    "v_exp_f32 v33, v33\n"
    "v_cvt_pk_bf16_f32 v58, v28, v29\n"
    "v_exp_f32 v34, v34\n"
    "ds_write2st64_b32 v228, v216, v217 offset0:76 offset1:77\n"
    "v_exp_f32 v35, v35\n"
    "v_cvt_pk_bf16_f32 v59, v30, v31\n"
    "v_exp_f32 v36, v36\n"
    "ds_write2st64_b32 v228, v218, v219 offset0:78 offset1:79\n"
    "v_exp_f32 v37, v37\n"
    "v_mfma_f32_32x32x16_bf16 v[0:15], v[96:99], v[56:59], v[0:15]\n"
    "v_cvt_pk_bf16_f32 v52, v32, v33\n"
    "v_exp_f32 v38, v38\n"
    "ds_write_b32 v228, v220 offset:20480\n"
    "v_exp_f32 v39, v39\n"
    "v_cvt_pk_bf16_f32 v53, v34, v35\n"
    "v_mfma_f32_32x32x16_bf16 v[16:31], v[68:71], v[48:51], 0\n"
    "v_exp_f32 v40, v40\n"
    "v_exp_f32 v41, v41\n"
    "v_cvt_pk_bf16_f32 v54, v36, v37\n"
    "v_exp_f32 v42, v42\n"
    "v_exp_f32 v43, v43\n"
    "v_cvt_pk_bf16_f32 v55, v38, v39\n"
    "v_exp_f32 v44, v44\n"
    "v_exp_f32 v45, v45\n"
    "v_mfma_f32_32x32x16_bf16 v[0:15], v[100:103], v[52:55], v[0:15]\n"
    "v_cvt_pk_bf16_f32 v56, v40, v41\n"
    "v_exp_f32 v46, v46\n"
    "v_exp_f32 v47, v47\n"
    "v_cvt_pk_bf16_f32 v57, v42, v43\n"
    "s_waitcnt lgkmcnt(0)\n"
    "s_barrier\n"
    "ds_read_b128 v[188:191], v232 offset:0\n"
    "ds_read_b128 v[192:195], v232 offset:1024\n"
    "v_exp_f32 v16, v16\n"
    "ds_read2st64_b32 v[220:221], v230 offset0:72 offset1:81\n"
    "v_exp_f32 v17, v17\n"
    "v_cvt_pk_bf16_f32 v58, v44, v45\n"
    "v_exp_f32 v18, v18\n"
    "ds_read2st64_b32 v[222:223], v230 offset0:90 offset1:99\n"
    "v_exp_f32 v19, v19\n"
    "v_cvt_pk_bf16_f32 v59, v46, v47\n"
    "v_exp_f32 v20, v20\n"
    "ds_read2st64_b32 v[224:225], v230 offset0:108 offset1:117\n"
    "v_exp_f32 v21, v21\n"
    "v_mfma_f32_32x32x16_bf16 v[0:15], v[104:107], v[56:59], v[0:15]\n"
    "v_cvt_pk_bf16_f32 v52, v16, v17\n"
    "v_exp_f32 v22, v22\n"
    "ds_read2st64_b32 v[226:227], v230 offset0:126 offset1:135\n"
    "v_exp_f32 v23, v23\n"
    "v_cvt_pk_bf16_f32 v53, v18, v19\n"
    "v_mfma_f32_32x32x16_bf16 v[32:47], v[72:75], v[48:51], 0\n"
    "v_exp_f32 v24, v24\n"
    "ds_read2st64_b32 v[212:213], v229 offset0:72 offset1:81\n"
    "v_exp_f32 v25, v25\n"
    "s_add_u32 %[st], %[rot], 3\n"
    "s_and_b32 %[st], %[st], 7\n"
    "s_lshl_b32 %[st], %[st], 10\n"
    "s_add_u32 %[st], %[st], %[sq]\n"
    "buffer_load_dwordx4 v[240:243], v234, %[rs], %[st] offen sc1\n"
    "v_cvt_pk_bf16_f32 v54, v20, v21\n"
    "v_exp_f32 v26, v26\n"
    "ds_read2st64_b32 v[214:215], v229 offset0:90 offset1:99\n"
    "v_exp_f32 v27, v27\n"
    "v_cvt_pk_bf16_f32 v55, v22, v23\n"
    "s_waitcnt vmcnt(11)\n"
    "s_waitcnt lgkmcnt(7)\n"
    "v_mfma_f32_16x16x32_bf16 v[196:199], v[156:159], v[188:191], 0\n"
    "s_add_u32 %[sa], %[rot], 1\n"
    "s_and_b32 %[sa], %[sa], 7\n"
    "s_lshl_b32 %[sa], %[sa], 16\n"
    "s_add_u32 %[sa], %[sa], %[swo]\n"
    "buffer_load_dwordx4 v[156:159], v234, %[rs], %[sa] offen offset:0 sc1\n"
    "v_exp_f32 v28, v28\n"
    "ds_read2st64_b32 v[216:217], v229 offset0:108 offset1:117\n"
    "v_exp_f32 v29, v29\n"
    "v_mfma_f32_32x32x16_bf16 v[0:15], v[108:111], v[52:55], v[0:15]\n"
    "v_cvt_pk_bf16_f32 v56, v24, v25\n"
    "v_exp_f32 v30, v30\n"
    "ds_read2st64_b32 v[218:219], v229 offset0:126 offset1:135\n"
    "v_exp_f32 v31, v31\n"
    "v_cvt_pk_bf16_f32 v57, v26, v27\n"
    "v_exp_f32 v32, v32\n"
    "v_exp_f32 v33, v33\n"
    "s_waitcnt lgkmcnt(4)\n"
    "v_add_f32 v220, v220, v221\n"
    "v_cvt_pk_bf16_f32 v58, v28, v29\n"
    "v_exp_f32 v34, v34\n"
    "v_exp_f32 v35, v35\n"
    "v_add_f32 v222, v222, v223\n"
    "v_cvt_pk_bf16_f32 v59, v30, v31\n"
    "v_exp_f32 v36, v36\n"
    "v_exp_f32 v37, v37\n"
    "v_add_f32 v224, v224, v225\n"
    "v_mfma_f32_32x32x16_bf16 v[0:15], v[112:115], v[56:59], v[0:15]\n"
    "v_cvt_pk_bf16_f32 v52, v32, v33\n"
    "v_exp_f32 v38, v38\n"
    "v_exp_f32 v39, v39\n"
    "v_add_f32 v226, v226, v227\n"
    "v_cvt_pk_bf16_f32 v53, v34, v35\n"
    "v_mfma_f32_32x32x16_bf16 v[16:31], v[76:79], v[48:51], 0\n"
    "v_exp_f32 v40, v40\n"
    "v_exp_f32 v41, v41\n"
    "v_add_f32 v220, v220, v222\n"
    "v_cvt_pk_bf16_f32 v54, v36, v37\n"
    "v_exp_f32 v42, v42\n"
    "v_exp_f32 v43, v43\n"
    "v_add_f32 v224, v224, v226\n"
    "v_cvt_pk_bf16_f32 v55, v38, v39\n"
    "v_exp_f32 v44, v44\n"
    "v_exp_f32 v45, v45\n"
    "s_waitcnt lgkmcnt(0)\n"
    "v_add_f32 v212, v212, v213\n"
    "v_mfma_f32_32x32x16_bf16 v[0:15], v[116:119], v[52:55], v[0:15]\n"
    "v_cvt_pk_bf16_f32 v56, v40, v41\n"
    "v_exp_f32 v46, v46\n"
    "v_exp_f32 v47, v47\n"
    "v_cvt_pk_bf16_f32 v57, v42, v43\n"
    "v_exp_f32 v16, v16\n"
    "v_exp_f32 v17, v17\n"
    "v_add_f32 v220, v220, v224\n"
    "v_cvt_pk_bf16_f32 v58, v44, v45\n"
    "v_exp_f32 v18, v18\n"
    "v_exp_f32 v19, v19\n"
    "v_add_f32 v214, v214, v215\n"
    "v_cvt_pk_bf16_f32 v59, v46, v47\n"
    "v_mfma_f32_16x16x32_bf16 v[200:203], v[164:167], v[188:191], 0\n"
    "buffer_load_dwordx4 v[164:167], v234, %[rs], %[sa] offen offset:2048 sc1\n"
    "v_exp_f32 v20, v20\n"
    "v_exp_f32 v21, v21\n"
    "v_rcp_f32 v221, v220\n"
    "v_mfma_f32_32x32x16_bf16 v[0:15], v[120:123], v[56:59], v[0:15]\n"
    "v_cvt_pk_bf16_f32 v52, v16, v17\n"
    "v_exp_f32 v22, v22\n"
    "v_exp_f32 v23, v23\n"
    "v_add_f32 v216, v216, v217\n"
    "v_cvt_pk_bf16_f32 v53, v18, v19\n"
    "v_mfma_f32_32x32x16_bf16 v[32:47], v[80:83], v[48:51], 0\n"
    "v_exp_f32 v24, v24\n"
    "v_exp_f32 v25, v25\n"
    "v_add_f32 v218, v218, v219\n"
    "s_add_u32 m0, %[wdst], 28672\n"
    "v_lshl_add_u64 v[236:237], v[236:237], 0, %[wstep]\n"
    "global_load_lds_dwordx4 v[236:237], off\n"
    "v_cvt_pk_bf16_f32 v54, v20, v21\n"
    "v_exp_f32 v26, v26\n"
    "v_exp_f32 v27, v27\n"
    "v_add_f32 v212, v212, v214\n"
    "v_cvt_pk_bf16_f32 v55, v22, v23\n"
    "s_waitcnt vmcnt(11)\n"
    "v_mfma_f32_16x16x32_bf16 v[204:207], v[172:175], v[188:191], 0\n"
    "s_add_u32 %[sb], %[rot], 1\n"
    "s_and_b32 %[sb], %[sb], 7\n"
    "s_lshl_b32 %[sb], %[sb], 16\n"
    "s_add_u32 %[sb], %[sb], %[swo]\n"
    "s_add_u32 %[sb], %[sb], 4096\n"
    "buffer_load_dwordx4 v[172:175], v234, %[rs], %[sb] offen offset:0 sc1\n"
    "v_exp_f32 v28, v28\n"
    "v_exp_f32 v29, v29\n"
    "v_add_f32 v216, v216, v218\n"
    "v_mfma_f32_32x32x16_bf16 v[0:15], v[124:127], v[52:55], v[0:15]\n"
    "v_cvt_pk_bf16_f32 v56, v24, v25\n"
    "v_exp_f32 v30, v30\n"
    "v_exp_f32 v31, v31\n"
    "v_cvt_pk_bf16_f32 v57, v26, v27\n"
    "v_exp_f32 v32, v32\n"
    "v_exp_f32 v33, v33\n"
    "v_min_u32 v238, v238, v220\n"
    "v_cvt_pk_bf16_f32 v58, v28, v29\n"
    "v_mfma_f32_16x16x32_bf16 v[208:211], v[180:183], v[188:191], 0\n"
    "buffer_load_dwordx4 v[180:183], v234, %[rs], %[sb] offen offset:2048 sc1\n"
    "v_exp_f32 v34, v34\n"
    "v_exp_f32 v35, v35\n"
    "v_add_f32 v212, v212, v216\n"
    "v_cvt_pk_bf16_f32 v59, v30, v31\n"
    "v_exp_f32 v36, v36\n"
    "v_exp_f32 v37, v37\n"
    "v_add_f32 v220, |v212|, v220\n"
    "v_mfma_f32_32x32x16_bf16 v[0:15], v[128:131], v[56:59], v[0:15]\n"
    "v_cvt_pk_bf16_f32 v52, v32, v33\n"
    "v_exp_f32 v38, v38\n"
    "v_exp_f32 v39, v39\n"
    "v_mul_f32 v212, v212, v221\n"
    "v_cvt_pk_bf16_f32 v53, v34, v35\n"
    "v_mfma_f32_32x32x16_bf16 v[16:31], v[84:87], v[48:51], 0\n"
    "v_exp_f32 v40, v40\n"
    "v_exp_f32 v41, v41\n"
    "v_max_u32 v239, v239, v220\n"
    "v_cvt_pk_bf16_f32 v54, v36, v37\n"
    "v_exp_f32 v42, v42\n"
    "v_exp_f32 v43, v43\n"
    "v_cvt_pk_bf16_f32 v212, v212, v212\n"
    "v_cvt_pk_bf16_f32 v55, v38, v39\n"
    "s_waitcnt vmcnt(11)\n"
    "v_mfma_f32_16x16x32_bf16 v[196:199], v[160:163], v[192:195], v[196:199]\n"
    "buffer_load_dwordx4 v[160:163], v234, %[rs], %[sa] offen offset:1024 sc1\n"
    "v_exp_f32 v44, v44\n"
    "v_exp_f32 v45, v45\n"
    "ds_write_b16 v231, v212 offset:2048\n"
    "v_mfma_f32_32x32x16_bf16 v[0:15], v[132:135], v[52:55], v[0:15]\n"
    "v_cvt_pk_bf16_f32 v56, v40, v41\n"
    "v_exp_f32 v46, v46\n"
    "v_exp_f32 v47, v47\n"
    "v_cvt_pk_bf16_f32 v57, v42, v43\n"
    "v_exp_f32 v16, v16\n"
    "v_exp_f32 v17, v17\n"
    "s_waitcnt vmcnt(11)\n"
    "s_mov_b64 exec, 1\n"
    "buffer_store_dwordx2 v[250:251], v248, %[rs], 0 offen sc1\n"
    "s_mov_b64 exec, -1\n"
    "v_cvt_pk_bf16_f32 v58, v44, v45\n"
    "v_mfma_f32_16x16x32_bf16 v[200:203], v[168:171], v[192:195], v[200:203]\n"
    "buffer_load_dwordx4 v[168:171], v234, %[rs], %[sa] offen offset:3072 sc1\n"
    "v_exp_f32 v18, v18\n"
    "v_exp_f32 v19, v19\n"
    "v_cvt_pk_bf16_f32 v59, v46, v47\n"
    "v_exp_f32 v20, v20\n"
    "v_exp_f32 v21, v21\n"
    "v_mfma_f32_32x32x16_bf16 v[0:15], v[136:139], v[56:59], v[0:15]\n"
    "v_cvt_pk_bf16_f32 v52, v16, v17\n"
    "v_exp_f32 v22, v22\n"
    "v_exp_f32 v23, v23\n"
    "v_cvt_pk_bf16_f32 v53, v18, v19\n"
    "v_mfma_f32_32x32x16_bf16 v[32:47], v[88:91], v[48:51], 0\n"
    "v_exp_f32 v24, v24\n"
    "v_exp_f32 v25, v25\n"
    "s_add_u32 m0, %[wdst], 35840\n"
    "v_lshl_add_u64 v[236:237], v[236:237], 0, %[wstep]\n"
    "global_load_lds_dwordx4 v[236:237], off\n"
    "v_cvt_pk_bf16_f32 v54, v20, v21\n"
    "v_exp_f32 v26, v26\n"
    "v_exp_f32 v27, v27\n"
    "v_cvt_pk_bf16_f32 v55, v22, v23\n"
    "v_exp_f32 v28, v28\n"
    "v_exp_f32 v29, v29\n"
    "v_mfma_f32_32x32x16_bf16 v[0:15], v[140:143], v[52:55], v[0:15]\n"
    "v_cvt_pk_bf16_f32 v56, v24, v25\n"
    "v_exp_f32 v30, v30\n"
    "v_exp_f32 v31, v31\n"
    "v_cvt_pk_bf16_f32 v57, v26, v27\n"
    "v_exp_f32 v32, v32\n"
    "v_exp_f32 v33, v33\n"
    "v_cvt_pk_bf16_f32 v58, v28, v29\n"
    "s_waitcnt vmcnt(11)\n"
    "v_mfma_f32_16x16x32_bf16 v[204:207], v[176:179], v[192:195], v[204:207]\n"
    "buffer_load_dwordx4 v[176:179], v234, %[rs], %[sb] offen offset:1024 sc1\n"
    "v_exp_f32 v34, v34\n"
    "v_exp_f32 v35, v35\n"
    "v_cvt_pk_bf16_f32 v59, v30, v31\n"
    "v_exp_f32 v36, v36\n"
    "v_exp_f32 v37, v37\n"
    "v_mfma_f32_32x32x16_bf16 v[0:15], v[144:147], v[56:59], v[0:15]\n"
    "v_cvt_pk_bf16_f32 v52, v32, v33\n"
    "v_exp_f32 v38, v38\n"
    "v_exp_f32 v39, v39\n"
    "v_cvt_pk_bf16_f32 v53, v34, v35\n"
    "s_waitcnt vmcnt(10)\n"
    "v_mfma_f32_32x32x16_bf16 v[16:31], v[60:63], v[240:243], 0\n"
    "v_exp_f32 v40, v40\n"
    "v_exp_f32 v41, v41\n"
    "v_cvt_pk_bf16_f32 v54, v36, v37\n"
    "v_exp_f32 v42, v42\n"
    "v_exp_f32 v43, v43\n"
    "v_cvt_pk_bf16_f32 v55, v38, v39\n"
    "v_mfma_f32_16x16x32_bf16 v[208:211], v[184:187], v[192:195], v[208:211]\n"
    "buffer_load_dwordx4 v[184:187], v234, %[rs], %[sb] offen offset:3072 sc1\n"
    "v_exp_f32 v44, v44\n"
    "v_exp_f32 v45, v45\n"
    "v_mfma_f32_32x32x16_bf16 v[0:15], v[148:151], v[52:55], v[0:15]\n"
    "v_cvt_pk_bf16_f32 v56, v40, v41\n"
    "v_exp_f32 v46, v46\n"
    "v_exp_f32 v47, v47\n"
    "v_cvt_pk_bf16_f32 v57, v42, v43\n"
    "v_exp_f32 v16, v16\n"
    "v_exp_f32 v17, v17\n"
    "v_cvt_pk_bf16_f32 v58, v44, v45\n"
    "v_exp_f32 v18, v18\n"
    "v_exp_f32 v19, v19\n"
    "v_cvt_pk_bf16_f32 v59, v46, v47\n"
    "v_exp_f32 v20, v20\n"
    "v_exp_f32 v21, v21\n"
    "v_mfma_f32_32x32x16_bf16 v[0:15], v[152:155], v[56:59], v[0:15]\n"
    "v_cvt_pk_bf16_f32 v52, v16, v17\n"
    "v_exp_f32 v22, v22\n"
    "v_exp_f32 v23, v23\n"
    "v_cvt_pk_bf16_f32 v53, v18, v19\n"
    "v_mfma_f32_32x32x16_bf16 v[32:47], v[64:67], v[240:243], 0\n"
    "v_exp_f32 v24, v24\n"
    "v_exp_f32 v25, v25\n"
    "s_add_u32 m0, %[wdst], 43008\n"
    "v_lshl_add_u64 v[236:237], v[236:237], 0, %[wstep]\n"
    "global_load_lds_dwordx4 v[236:237], off\n"
    "v_cvt_pk_bf16_f32 v54, v20, v21\n"
    "v_exp_f32 v26, v26\n"
    "v_exp_f32 v27, v27\n"
    "v_cvt_pk_bf16_f32 v55, v22, v23\n"
    "v_exp_f32 v28, v28\n"
    "v_exp_f32 v29, v29\n"
    "v_mfma_f32_32x32x16_bf16 v[212:227], v[92:95], v[52:55], 0\n"
    "v_cvt_pk_bf16_f32 v56, v24, v25\n"
    "v_exp_f32 v30, v30\n"
    "ds_write2st64_b32 v228, v0, v1 offset0:0 offset1:1\n"
    "v_exp_f32 v31, v31\n"
    "v_cvt_pk_bf16_f32 v57, v26, v27\n"
    "v_exp_f32 v32, v32\n"
    "ds_write2st64_b32 v228, v2, v3 offset0:2 offset1:3\n"
    "v_exp_f32 v33, v33\n"
    "v_cvt_pk_bf16_f32 v58, v28, v29\n"
    "v_exp_f32 v34, v34\n"
    "ds_write2st64_b32 v228, v4, v5 offset0:4 offset1:5\n"
    "v_exp_f32 v35, v35\n"
    "v_cvt_pk_bf16_f32 v59, v30, v31\n"
    "v_exp_f32 v36, v36\n"
    "ds_write2st64_b32 v228, v6, v7 offset0:6 offset1:7\n"
    "v_exp_f32 v37, v37\n"
    "v_mfma_f32_32x32x16_bf16 v[212:227], v[96:99], v[56:59], v[212:227]\n"
    "v_cvt_pk_bf16_f32 v52, v32, v33\n"
    "v_exp_f32 v38, v38\n"
    "ds_write_b32 v228, v8 offset:2048\n"
    "v_exp_f32 v39, v39\n"
    "v_cvt_pk_bf16_f32 v53, v34, v35\n"
    "v_mfma_f32_32x32x16_bf16 v[16:31], v[68:71], v[240:243], 0\n"
    "v_exp_f32 v40, v40\n"
    "v_exp_f32 v41, v41\n"
    "v_cvt_pk_bf16_f32 v54, v36, v37\n"
    "v_exp_f32 v42, v42\n"
    "v_exp_f32 v43, v43\n"
    "v_cvt_pk_bf16_f32 v55, v38, v39\n"
    "v_exp_f32 v44, v44\n"
    "v_exp_f32 v45, v45\n"
    "v_mfma_f32_32x32x16_bf16 v[212:227], v[100:103], v[52:55], v[212:227]\n"
    "v_cvt_pk_bf16_f32 v56, v40, v41\n"
    "v_exp_f32 v46, v46\n"
    "v_exp_f32 v47, v47\n"
    "v_cvt_pk_bf16_f32 v57, v42, v43\n"
    "s_waitcnt lgkmcnt(0)\n"
    "s_barrier\n"
    "ds_read_b128 v[188:191], v232 offset:2048\n"
    "ds_read_b128 v[192:195], v232 offset:3072\n"
    "v_exp_f32 v16, v16\n"
    "ds_read2st64_b32 v[8:9], v230 offset0:0 offset1:9\n"
    "v_exp_f32 v17, v17\n"
    "v_cvt_pk_bf16_f32 v58, v44, v45\n"
    "v_exp_f32 v18, v18\n"
    "ds_read2st64_b32 v[10:11], v230 offset0:18 offset1:27\n"
    "v_exp_f32 v19, v19\n"
    "v_cvt_pk_bf16_f32 v59, v46, v47\n"
    "v_exp_f32 v20, v20\n"
    "ds_read2st64_b32 v[12:13], v230 offset0:36 offset1:45\n"
    "v_exp_f32 v21, v21\n"
    "v_mfma_f32_32x32x16_bf16 v[212:227], v[104:107], v[56:59], v[212:227]\n"
    "v_cvt_pk_bf16_f32 v52, v16, v17\n"
    "v_exp_f32 v22, v22\n"
    "ds_read2st64_b32 v[14:15], v230 offset0:54 offset1:63\n"
    "v_exp_f32 v23, v23\n"
    "v_cvt_pk_bf16_f32 v53, v18, v19\n"
    "v_mfma_f32_32x32x16_bf16 v[32:47], v[72:75], v[240:243], 0\n"
    "v_exp_f32 v24, v24\n"
    "ds_read2st64_b32 v[0:1], v229 offset0:0 offset1:9\n"
    "v_exp_f32 v25, v25\n"
    "s_add_u32 %[st], %[rot], 4\n"
    "s_and_b32 %[st], %[st], 7\n"
    "s_lshl_b32 %[st], %[st], 10\n"
    "s_add_u32 %[st], %[st], %[sq]\n"
    "buffer_load_dwordx4 v[48:51], v234, %[rs], %[st] offen sc1\n"
    "v_cvt_pk_bf16_f32 v54, v20, v21\n"
    "v_exp_f32 v26, v26\n"
    "ds_read2st64_b32 v[2:3], v229 offset0:18 offset1:27\n"
    "v_exp_f32 v27, v27\n"
    "v_cvt_pk_bf16_f32 v55, v22, v23\n"
    "s_waitcnt vmcnt(11)\n"
    "s_waitcnt lgkmcnt(7)\n"
    "v_mfma_f32_16x16x32_bf16 v[196:199], v[156:159], v[188:191], v[196:199]\n"
    "s_add_u32 %[sa], %[rot], 2\n"
    "s_and_b32 %[sa], %[sa], 7\n"
    "s_lshl_b32 %[sa], %[sa], 16\n"
    "s_add_u32 %[sa], %[sa], %[swo]\n"
    "buffer_load_dwordx4 v[156:159], v234, %[rs], %[sa] offen offset:0 sc1\n"
    "v_exp_f32 v28, v28\n"
    "ds_read2st64_b32 v[4:5], v229 offset0:36 offset1:45\n"
    "v_exp_f32 v29, v29\n"
    "v_mfma_f32_32x32x16_bf16 v[212:227], v[108:111], v[52:55], v[212:227]\n"
    "v_cvt_pk_bf16_f32 v56, v24, v25\n"
    "v_exp_f32 v30, v30\n"
    "ds_read2st64_b32 v[6:7], v229 offset0:54 offset1:63\n"
    "v_exp_f32 v31, v31\n"
    "v_cvt_pk_bf16_f32 v57, v26, v27\n"
    "v_exp_f32 v32, v32\n"
    "v_exp_f32 v33, v33\n"
    "s_waitcnt lgkmcnt(4)\n"
    "v_add_f32 v8, v8, v9\n"
    "v_cvt_pk_bf16_f32 v58, v28, v29\n"
    "v_exp_f32 v34, v34\n"
    "v_exp_f32 v35, v35\n"
    "v_add_f32 v10, v10, v11\n"
    "v_cvt_pk_bf16_f32 v59, v30, v31\n"
    "v_exp_f32 v36, v36\n"
    "v_exp_f32 v37, v37\n"
    "v_add_f32 v12, v12, v13\n"
    "v_mfma_f32_32x32x16_bf16 v[212:227], v[112:115], v[56:59], v[212:227]\n"
    "v_cvt_pk_bf16_f32 v52, v32, v33\n"
    "v_exp_f32 v38, v38\n"
    "v_exp_f32 v39, v39\n"
    "v_add_f32 v14, v14, v15\n"
    "v_cvt_pk_bf16_f32 v53, v34, v35\n"
    "v_mfma_f32_32x32x16_bf16 v[16:31], v[76:79], v[240:243], 0\n"
    "v_exp_f32 v40, v40\n"
    "v_exp_f32 v41, v41\n"
    "v_add_f32 v8, v8, v10\n"
    "v_cvt_pk_bf16_f32 v54, v36, v37\n"
    "v_exp_f32 v42, v42\n"
    "v_exp_f32 v43, v43\n"
    "v_add_f32 v12, v12, v14\n"
    "v_cvt_pk_bf16_f32 v55, v38, v39\n"
    "v_exp_f32 v44, v44\n"
    "v_exp_f32 v45, v45\n"
    "s_waitcnt lgkmcnt(0)\n"
    "v_add_f32 v0, v0, v1\n"
    "v_mfma_f32_32x32x16_bf16 v[212:227], v[116:119], v[52:55], v[212:227]\n"
    "v_cvt_pk_bf16_f32 v56, v40, v41\n"
    "v_exp_f32 v46, v46\n"
    "v_exp_f32 v47, v47\n"
    "v_cvt_pk_bf16_f32 v57, v42, v43\n"
    "v_exp_f32 v16, v16\n"
    "v_exp_f32 v17, v17\n"
    "v_add_f32 v8, v8, v12\n"
    "v_cvt_pk_bf16_f32 v58, v44, v45\n"
    "v_exp_f32 v18, v18\n"
    "v_exp_f32 v19, v19\n"
    "v_add_f32 v2, v2, v3\n"
    "v_cvt_pk_bf16_f32 v59, v46, v47\n"
    "v_mfma_f32_16x16x32_bf16 v[200:203], v[164:167], v[188:191], v[200:203]\n"
    "buffer_load_dwordx4 v[164:167], v234, %[rs], %[sa] offen offset:2048 sc1\n"
    "v_exp_f32 v20, v20\n"
    "v_exp_f32 v21, v21\n"
    "v_rcp_f32 v9, v8\n"
    "v_mfma_f32_32x32x16_bf16 v[212:227], v[120:123], v[56:59], v[212:227]\n"
    "v_cvt_pk_bf16_f32 v52, v16, v17\n"
    "v_exp_f32 v22, v22\n"
    "v_exp_f32 v23, v23\n"
    "v_add_f32 v4, v4, v5\n"
    "v_cvt_pk_bf16_f32 v53, v18, v19\n"
    "v_mfma_f32_32x32x16_bf16 v[32:47], v[80:83], v[240:243], 0\n"
    "v_exp_f32 v24, v24\n"
    "v_exp_f32 v25, v25\n"
    "v_add_f32 v6, v6, v7\n"
    "s_add_u32 m0, %[wdst], 50176\n"
    "v_lshl_add_u64 v[236:237], v[236:237], 0, %[wstep]\n"
    "global_load_lds_dwordx4 v[236:237], off\n"
    "v_cvt_pk_bf16_f32 v54, v20, v21\n"
    "v_exp_f32 v26, v26\n"
    "v_exp_f32 v27, v27\n"
    "v_add_f32 v0, v0, v2\n"
    "v_cvt_pk_bf16_f32 v55, v22, v23\n"
    "s_waitcnt vmcnt(11)\n"
    "v_mfma_f32_16x16x32_bf16 v[204:207], v[172:175], v[188:191], v[204:207]\n"
    "s_add_u32 %[sb], %[rot], 2\n"
    "s_and_b32 %[sb], %[sb], 7\n"
    "s_lshl_b32 %[sb], %[sb], 16\n"
    "s_add_u32 %[sb], %[sb], %[swo]\n"
    "s_add_u32 %[sb], %[sb], 4096\n"
    "buffer_load_dwordx4 v[172:175], v234, %[rs], %[sb] offen offset:0 sc1\n"
    "v_exp_f32 v28, v28\n"
    "v_exp_f32 v29, v29\n"
    "v_add_f32 v4, v4, v6\n"
    "v_mfma_f32_32x32x16_bf16 v[212:227], v[124:127], v[52:55], v[212:227]\n"
    "v_cvt_pk_bf16_f32 v56, v24, v25\n"
    "v_exp_f32 v30, v30\n"
    "v_exp_f32 v31, v31\n"
    "v_cvt_pk_bf16_f32 v57, v26, v27\n"
    "v_exp_f32 v32, v32\n"
    "v_exp_f32 v33, v33\n"
    "v_min_u32 v238, v238, v8\n"
    "v_cvt_pk_bf16_f32 v58, v28, v29\n"
    "v_mfma_f32_16x16x32_bf16 v[208:211], v[180:183], v[188:191], v[208:211]\n"
    "buffer_load_dwordx4 v[180:183], v234, %[rs], %[sb] offen offset:2048 sc1\n"
    "v_exp_f32 v34, v34\n"
    "v_exp_f32 v35, v35\n"
    "v_add_f32 v0, v0, v4\n"
    "v_cvt_pk_bf16_f32 v59, v30, v31\n"
    "v_exp_f32 v36, v36\n"
    "v_exp_f32 v37, v37\n"
    "v_add_f32 v8, |v0|, v8\n"
    "v_mfma_f32_32x32x16_bf16 v[212:227], v[128:131], v[56:59], v[212:227]\n"
    "v_cvt_pk_bf16_f32 v52, v32, v33\n"
    "v_exp_f32 v38, v38\n"
    "v_exp_f32 v39, v39\n"
    "v_mul_f32 v0, v0, v9\n"
    "v_cvt_pk_bf16_f32 v53, v34, v35\n"
    "v_mfma_f32_32x32x16_bf16 v[16:31], v[84:87], v[240:243], 0\n"
    "v_exp_f32 v40, v40\n"
    "v_exp_f32 v41, v41\n"
    "v_max_u32 v239, v239, v8\n"
    "v_cvt_pk_bf16_f32 v54, v36, v37\n"
    "v_exp_f32 v42, v42\n"
    "v_exp_f32 v43, v43\n"
    "v_cvt_pk_bf16_f32 v0, v0, v0\n"
    "v_cvt_pk_bf16_f32 v55, v38, v39\n"
    "s_waitcnt vmcnt(10)\n"
    "v_mfma_f32_16x16x32_bf16 v[196:199], v[160:163], v[192:195], v[196:199]\n"
    "buffer_load_dwordx4 v[160:163], v234, %[rs], %[sa] offen offset:1024 sc1\n"
    "v_exp_f32 v44, v44\n"
    "v_exp_f32 v45, v45\n"
    "ds_write_b16 v231, v0 offset:0\n"
    "v_mfma_f32_32x32x16_bf16 v[212:227], v[132:135], v[52:55], v[212:227]\n"
    "v_cvt_pk_bf16_f32 v56, v40, v41\n"
    "v_exp_f32 v46, v46\n"
    "v_exp_f32 v47, v47\n"
    "v_cvt_pk_bf16_f32 v57, v42, v43\n"
    "v_exp_f32 v16, v16\n"
    "v_exp_f32 v17, v17\n"
    "v_cvt_pk_bf16_f32 v58, v44, v45\n"
    "v_mfma_f32_16x16x32_bf16 v[200:203], v[168:171], v[192:195], v[200:203]\n"
    "buffer_load_dwordx4 v[168:171], v234, %[rs], %[sa] offen offset:3072 sc1\n"
    "v_exp_f32 v18, v18\n"
    "v_exp_f32 v19, v19\n"
    "v_cvt_pk_bf16_f32 v59, v46, v47\n"
    "v_exp_f32 v20, v20\n"
    "v_exp_f32 v21, v21\n"
    "v_mfma_f32_32x32x16_bf16 v[212:227], v[136:139], v[56:59], v[212:227]\n"
    "v_cvt_pk_bf16_f32 v52, v16, v17\n"
    "v_exp_f32 v22, v22\n"
    "v_exp_f32 v23, v23\n"
    "v_cvt_pk_bf16_f32 v53, v18, v19\n"
    "v_mfma_f32_32x32x16_bf16 v[32:47], v[88:91], v[240:243], 0\n"
    "v_exp_f32 v24, v24\n"
    "v_exp_f32 v25, v25\n"
    "s_add_u32 m0, %[wdst], 57344\n"
    "v_lshl_add_u64 v[236:237], v[236:237], 0, %[wstep]\n"
    "global_load_lds_dwordx4 v[236:237], off\n"
    "v_cvt_pk_bf16_f32 v54, v20, v21\n"
    "v_exp_f32 v26, v26\n"
    "v_exp_f32 v27, v27\n"
    "v_cvt_pk_bf16_f32 v55, v22, v23\n"
    "v_exp_f32 v28, v28\n"
    "v_exp_f32 v29, v29\n"
    "v_mfma_f32_32x32x16_bf16 v[212:227], v[140:143], v[52:55], v[212:227]\n"
    "v_cvt_pk_bf16_f32 v56, v24, v25\n"
    "v_exp_f32 v30, v30\n"
    "v_exp_f32 v31, v31\n"
    "v_cvt_pk_bf16_f32 v57, v26, v27\n"
    "v_exp_f32 v32, v32\n"
    "v_exp_f32 v33, v33\n"
    "v_cvt_pk_bf16_f32 v58, v28, v29\n"
    "s_waitcnt vmcnt(10)\n"
    "v_mfma_f32_16x16x32_bf16 v[204:207], v[176:179], v[192:195], v[204:207]\n"
    "buffer_load_dwordx4 v[176:179], v234, %[rs], %[sb] offen offset:1024 sc1\n"
    "v_exp_f32 v34, v34\n"
    "v_exp_f32 v35, v35\n"
    "v_lshlrev_b32 v247, 3, v234\n"
    "v_cvt_pk_bf16_f32 v59, v30, v31\n"
    "v_exp_f32 v36, v36\n"
    "v_exp_f32 v37, v37\n"
    "v_mfma_f32_32x32x16_bf16 v[212:227], v[144:147], v[56:59], v[212:227]\n"
    "v_cvt_pk_bf16_f32 v52, v32, v33\n"
    "v_exp_f32 v38, v38\n"
    "v_exp_f32 v39, v39\n"
    "v_cvt_pk_bf16_f32 v53, v34, v35\n"
    "s_waitcnt vmcnt(9)\n"
    "v_mfma_f32_32x32x16_bf16 v[16:31], v[60:63], v[48:51], 0\n"
    "v_exp_f32 v40, v40\n"
    "v_exp_f32 v41, v41\n"
    "s_mov_b64 exec, 0xffff\n"
    "buffer_load_dword v248, v247, %[rs], %[spf] offen sc1\n"
    "s_mov_b64 exec, -1\n"
    "v_cvt_pk_bf16_f32 v54, v36, v37\n"
    "v_exp_f32 v42, v42\n"
    "v_exp_f32 v43, v43\n"
    "v_cvt_pk_bf16_f32 v55, v38, v39\n"
    "v_mfma_f32_16x16x32_bf16 v[208:211], v[184:187], v[192:195], v[208:211]\n"
    "buffer_load_dwordx4 v[184:187], v234, %[rs], %[sb] offen offset:3072 sc1\n"
    "v_exp_f32 v44, v44\n"
    "v_exp_f32 v45, v45\n"
    "v_mfma_f32_32x32x16_bf16 v[212:227], v[148:151], v[52:55], v[212:227]\n"
    "v_cvt_pk_bf16_f32 v56, v40, v41\n"
    "v_exp_f32 v46, v46\n"
    "v_exp_f32 v47, v47\n"
    "v_cvt_pk_bf16_f32 v57, v42, v43\n"
    "v_exp_f32 v16, v16\n"
    "v_exp_f32 v17, v17\n"
    "v_cvt_pk_bf16_f32 v58, v44, v45\n"
    "v_exp_f32 v18, v18\n"
    "v_exp_f32 v19, v19\n"
    "v_cvt_pk_bf16_f32 v59, v46, v47\n"
    "v_exp_f32 v20, v20\n"
    "v_exp_f32 v21, v21\n"
    "v_mfma_f32_32x32x16_bf16 v[212:227], v[152:155], v[56:59], v[212:227]\n"
    "v_cvt_pk_bf16_f32 v52, v16, v17\n"
    "v_exp_f32 v22, v22\n"
    "v_exp_f32 v23, v23\n"
    "v_cvt_pk_bf16_f32 v53, v18, v19\n"
    "v_mfma_f32_32x32x16_bf16 v[32:47], v[64:67], v[48:51], 0\n"
    "v_exp_f32 v24, v24\n"
    "v_exp_f32 v25, v25\n"
    "s_add_u32 m0, %[wdst], 64512\n"
    "v_lshl_add_u64 v[236:237], v[236:237], 0, %[wstep]\n"
    "global_load_lds_dwordx4 v[236:237], off\n"
    "v_cvt_pk_bf16_f32 v54, v20, v21\n"
    "v_exp_f32 v26, v26\n"
    "v_exp_f32 v27, v27\n"
    "v_cvt_pk_bf16_f32 v55, v22, v23\n"
    "v_exp_f32 v28, v28\n"
    "v_exp_f32 v29, v29\n"
    "v_mfma_f32_32x32x16_bf16 v[0:15], v[92:95], v[52:55], 0\n"
    "v_cvt_pk_bf16_f32 v56, v24, v25\n"
    "v_exp_f32 v30, v30\n"
    "ds_write2st64_b32 v228, v212, v213 offset0:72 offset1:73\n"
    "v_exp_f32 v31, v31\n"
    "v_cvt_pk_bf16_f32 v57, v26, v27\n"
    "v_exp_f32 v32, v32\n"
    "ds_write2st64_b32 v228, v214, v215 offset0:74 offset1:75\n"
    "v_exp_f32 v33, v33\n"
    "v_cvt_pk_bf16_f32 v58, v28, v29\n"
    "v_exp_f32 v34, v34\n"
    "ds_write2st64_b32 v228, v216, v217 offset0:76 offset1:77\n"
    "v_exp_f32 v35, v35\n"
    "v_cvt_pk_bf16_f32 v59, v30, v31\n"
    "v_exp_f32 v36, v36\n"
    "ds_write2st64_b32 v228, v218, v219 offset0:78 offset1:79\n"
    "v_exp_f32 v37, v37\n"
    "v_mfma_f32_32x32x16_bf16 v[0:15], v[96:99], v[56:59], v[0:15]\n"
    "v_cvt_pk_bf16_f32 v52, v32, v33\n"
    "v_exp_f32 v38, v38\n"
    "ds_write_b32 v228, v220 offset:20480\n"
    "v_exp_f32 v39, v39\n"
    "v_cvt_pk_bf16_f32 v53, v34, v35\n"
    "v_mfma_f32_32x32x16_bf16 v[16:31], v[68:71], v[48:51], 0\n"
    "v_exp_f32 v40, v40\n"
    "v_exp_f32 v41, v41\n"
    "v_cvt_pk_bf16_f32 v54, v36, v37\n"
    "v_exp_f32 v42, v42\n"
    "v_exp_f32 v43, v43\n"
    "v_cvt_pk_bf16_f32 v55, v38, v39\n"
    "v_exp_f32 v44, v44\n"
    "v_exp_f32 v45, v45\n"
    "v_mfma_f32_32x32x16_bf16 v[0:15], v[100:103], v[52:55], v[0:15]\n"
    "v_cvt_pk_bf16_f32 v56, v40, v41\n"
    "v_exp_f32 v46, v46\n"
    "v_exp_f32 v47, v47\n"
    "v_cvt_pk_bf16_f32 v57, v42, v43\n"
    "s_waitcnt lgkmcnt(0)\n"
    "s_barrier\n"
    "ds_read_b128 v[188:191], v232 offset:0\n"
    "ds_read_b128 v[192:195], v232 offset:1024\n"
    "v_exp_f32 v16, v16\n"
    "ds_read2st64_b32 v[220:221], v230 offset0:72 offset1:81\n"
    "v_exp_f32 v17, v17\n"
    "v_cvt_pk_bf16_f32 v58, v44, v45\n"
    "v_exp_f32 v18, v18\n"
    "ds_read2st64_b32 v[222:223], v230 offset0:90 offset1:99\n"
    "v_exp_f32 v19, v19\n"
    "v_cvt_pk_bf16_f32 v59, v46, v47\n"
    "v_exp_f32 v20, v20\n"
    "ds_read2st64_b32 v[224:225], v230 offset0:108 offset1:117\n"
    "v_exp_f32 v21, v21\n"
    "v_mfma_f32_32x32x16_bf16 v[0:15], v[104:107], v[56:59], v[0:15]\n"
    "v_cvt_pk_bf16_f32 v52, v16, v17\n"
    "v_exp_f32 v22, v22\n"
    "ds_read2st64_b32 v[226:227], v230 offset0:126 offset1:135\n"
    "v_exp_f32 v23, v23\n"
    "v_cvt_pk_bf16_f32 v53, v18, v19\n"
    "v_mfma_f32_32x32x16_bf16 v[32:47], v[72:75], v[48:51], 0\n"
    "v_exp_f32 v24, v24\n"
    "ds_read2st64_b32 v[212:213], v229 offset0:72 offset1:81\n"
    "v_exp_f32 v25, v25\n"
    "s_add_u32 %[st], %[rot], 5\n"
    "s_and_b32 %[st], %[st], 7\n"
    "s_lshl_b32 %[st], %[st], 10\n"
    "s_add_u32 %[st], %[st], %[sq]\n"
    "buffer_load_dwordx4 v[240:243], v234, %[rs], %[st] offen sc1\n"
    "v_cvt_pk_bf16_f32 v54, v20, v21\n"
    "v_exp_f32 v26, v26\n"
    "ds_read2st64_b32 v[214:215], v229 offset0:90 offset1:99\n"
    "v_exp_f32 v27, v27\n"
    "v_cvt_pk_bf16_f32 v55, v22, v23\n"
    "s_waitcnt vmcnt(11)\n"
    "s_waitcnt lgkmcnt(7)\n"
    "v_mfma_f32_16x16x32_bf16 v[196:199], v[156:159], v[188:191], v[196:199]\n"
    "s_add_u32 %[sa], %[rot], 3\n"
    "s_and_b32 %[sa], %[sa], 7\n"
    "s_lshl_b32 %[sa], %[sa], 16\n"
    "s_add_u32 %[sa], %[sa], %[swo]\n"
    "buffer_load_dwordx4 v[156:159], v234, %[rs], %[sa] offen offset:0 sc1\n"
    "v_exp_f32 v28, v28\n"
    "ds_read2st64_b32 v[216:217], v229 offset0:108 offset1:117\n"
    "v_exp_f32 v29, v29\n"
    "v_mfma_f32_32x32x16_bf16 v[0:15], v[108:111], v[52:55], v[0:15]\n"
    "v_cvt_pk_bf16_f32 v56, v24, v25\n"
    "v_exp_f32 v30, v30\n"
    "ds_read2st64_b32 v[218:219], v229 offset0:126 offset1:135\n"
    "v_exp_f32 v31, v31\n"
    "v_cvt_pk_bf16_f32 v57, v26, v27\n"
    "v_exp_f32 v32, v32\n"
    "v_exp_f32 v33, v33\n"
    "s_waitcnt lgkmcnt(4)\n"
    "v_add_f32 v220, v220, v221\n"
    "v_cvt_pk_bf16_f32 v58, v28, v29\n"
    "v_exp_f32 v34, v34\n"
    "v_exp_f32 v35, v35\n"
    "v_add_f32 v222, v222, v223\n"
    "v_cvt_pk_bf16_f32 v59, v30, v31\n"
    "v_exp_f32 v36, v36\n"
    "v_exp_f32 v37, v37\n"
    "v_add_f32 v224, v224, v225\n"
    "v_mfma_f32_32x32x16_bf16 v[0:15], v[112:115], v[56:59], v[0:15]\n"
    "v_cvt_pk_bf16_f32 v52, v32, v33\n"
    "v_exp_f32 v38, v38\n"
    "v_exp_f32 v39, v39\n"
    "v_add_f32 v226, v226, v227\n"
    "v_cvt_pk_bf16_f32 v53, v34, v35\n"
    "v_mfma_f32_32x32x16_bf16 v[16:31], v[76:79], v[48:51], 0\n"
    "v_exp_f32 v40, v40\n"
    "v_exp_f32 v41, v41\n"
    "v_add_f32 v220, v220, v222\n"
    "v_cvt_pk_bf16_f32 v54, v36, v37\n"
    "v_exp_f32 v42, v42\n"
    "v_exp_f32 v43, v43\n"
    "v_add_f32 v224, v224, v226\n"
    "v_cvt_pk_bf16_f32 v55, v38, v39\n"
    "v_exp_f32 v44, v44\n"
    "v_exp_f32 v45, v45\n"
    "s_waitcnt lgkmcnt(0)\n"
    "v_add_f32 v212, v212, v213\n"
    "v_mfma_f32_32x32x16_bf16 v[0:15], v[116:119], v[52:55], v[0:15]\n"
    "v_cvt_pk_bf16_f32 v56, v40, v41\n"
    "v_exp_f32 v46, v46\n"
    "v_exp_f32 v47, v47\n"
    "v_cvt_pk_bf16_f32 v57, v42, v43\n"
    "v_exp_f32 v16, v16\n"
    "v_exp_f32 v17, v17\n"
    "v_add_f32 v220, v220, v224\n"
    "v_cvt_pk_bf16_f32 v58, v44, v45\n"
    "v_exp_f32 v18, v18\n"
    "v_exp_f32 v19, v19\n"
    "v_add_f32 v214, v214, v215\n"
    "v_cvt_pk_bf16_f32 v59, v46, v47\n"
    "v_mfma_f32_16x16x32_bf16 v[200:203], v[164:167], v[188:191], v[200:203]\n"
    "buffer_load_dwordx4 v[164:167], v234, %[rs], %[sa] offen offset:2048 sc1\n"
    "v_exp_f32 v20, v20\n"
    "v_exp_f32 v21, v21\n"
    "v_rcp_f32 v221, v220\n"
    "v_mfma_f32_32x32x16_bf16 v[0:15], v[120:123], v[56:59], v[0:15]\n"
    "v_cvt_pk_bf16_f32 v52, v16, v17\n"
    "v_exp_f32 v22, v22\n"
    "v_exp_f32 v23, v23\n"
    "v_add_f32 v216, v216, v217\n"
    "v_cvt_pk_bf16_f32 v53, v18, v19\n"
    "v_mfma_f32_32x32x16_bf16 v[32:47], v[80:83], v[48:51], 0\n"
    "v_exp_f32 v24, v24\n"
    "v_exp_f32 v25, v25\n"
    "v_add_f32 v218, v218, v219\n"
    "s_add_u32 m0, %[wdst], 71680\n"
    "v_lshl_add_u64 v[236:237], v[236:237], 0, %[wstep]\n"
    "global_load_lds_dwordx4 v[236:237], off\n"
    "v_cvt_pk_bf16_f32 v54, v20, v21\n"
    "v_exp_f32 v26, v26\n"
    "v_exp_f32 v27, v27\n"
    "v_add_f32 v212, v212, v214\n"
    "v_cvt_pk_bf16_f32 v55, v22, v23\n"
    "s_waitcnt vmcnt(11)\n"
    "v_mfma_f32_16x16x32_bf16 v[204:207], v[172:175], v[188:191], v[204:207]\n"
    "s_add_u32 %[sb], %[rot], 3\n"
    "s_and_b32 %[sb], %[sb], 7\n"
    "s_lshl_b32 %[sb], %[sb], 16\n"
    "s_add_u32 %[sb], %[sb], %[swo]\n"
    "s_add_u32 %[sb], %[sb], 4096\n"
    "buffer_load_dwordx4 v[172:175], v234, %[rs], %[sb] offen offset:0 sc1\n"
    "v_exp_f32 v28, v28\n"
    "v_exp_f32 v29, v29\n"
    "v_add_f32 v216, v216, v218\n"
    "v_mfma_f32_32x32x16_bf16 v[0:15], v[124:127], v[52:55], v[0:15]\n"
    "v_cvt_pk_bf16_f32 v56, v24, v25\n"
    "v_exp_f32 v30, v30\n"
    "v_exp_f32 v31, v31\n"
    "v_cvt_pk_bf16_f32 v57, v26, v27\n"
    "v_exp_f32 v32, v32\n"
    "v_exp_f32 v33, v33\n"
    "v_min_u32 v238, v238, v220\n"
    "v_cvt_pk_bf16_f32 v58, v28, v29\n"
    "v_mfma_f32_16x16x32_bf16 v[208:211], v[180:183], v[188:191], v[208:211]\n"
    "buffer_load_dwordx4 v[180:183], v234, %[rs], %[sb] offen offset:2048 sc1\n"
    "v_exp_f32 v34, v34\n"
    "v_exp_f32 v35, v35\n"
    "v_add_f32 v212, v212, v216\n"
    "v_cvt_pk_bf16_f32 v59, v30, v31\n"
    "v_exp_f32 v36, v36\n"
    "v_exp_f32 v37, v37\n"
    "v_add_f32 v220, |v212|, v220\n"
    "v_mfma_f32_32x32x16_bf16 v[0:15], v[128:131], v[56:59], v[0:15]\n"
    "v_cvt_pk_bf16_f32 v52, v32, v33\n"
    "v_exp_f32 v38, v38\n"
    "v_exp_f32 v39, v39\n"
    "v_mul_f32 v212, v212, v221\n"
    "v_cvt_pk_bf16_f32 v53, v34, v35\n"
    "v_mfma_f32_32x32x16_bf16 v[16:31], v[84:87], v[48:51], 0\n"
    "v_exp_f32 v40, v40\n"
    "v_exp_f32 v41, v41\n"
    "v_max_u32 v239, v239, v220\n"
    "v_cvt_pk_bf16_f32 v54, v36, v37\n"
    "v_exp_f32 v42, v42\n"
    "v_exp_f32 v43, v43\n"
    "v_cvt_pk_bf16_f32 v212, v212, v212\n"
    "v_cvt_pk_bf16_f32 v55, v38, v39\n"
    "s_waitcnt vmcnt(11)\n"
    "v_mfma_f32_16x16x32_bf16 v[196:199], v[160:163], v[192:195], v[196:199]\n"
    "buffer_load_dwordx4 v[160:163], v234, %[rs], %[sa] offen offset:1024 sc1\n"
    "v_exp_f32 v44, v44\n"
    "v_exp_f32 v45, v45\n"
    "ds_write_b16 v231, v212 offset:2048\n"
    "v_mfma_f32_32x32x16_bf16 v[0:15], v[132:135], v[52:55], v[0:15]\n"
    "v_cvt_pk_bf16_f32 v56, v40, v41\n"
    "v_exp_f32 v46, v46\n"
    "v_exp_f32 v47, v47\n"
    "v_cvt_pk_bf16_f32 v57, v42, v43\n"
    "v_exp_f32 v16, v16\n"
    "v_exp_f32 v17, v17\n"
    "v_cvt_pk_bf16_f32 v58, v44, v45\n"
    "v_mfma_f32_16x16x32_bf16 v[200:203], v[168:171], v[192:195], v[200:203]\n"
    "buffer_load_dwordx4 v[168:171], v234, %[rs], %[sa] offen offset:3072 sc1\n"
    "v_exp_f32 v18, v18\n"
    "v_exp_f32 v19, v19\n"
    "v_cvt_pk_bf16_f32 v59, v46, v47\n"
    "v_exp_f32 v20, v20\n"
    "v_exp_f32 v21, v21\n"
    "v_mfma_f32_32x32x16_bf16 v[0:15], v[136:139], v[56:59], v[0:15]\n"
    "v_cvt_pk_bf16_f32 v52, v16, v17\n"
    "v_exp_f32 v22, v22\n"
    "v_exp_f32 v23, v23\n"
    "v_cvt_pk_bf16_f32 v53, v18, v19\n"
    "v_mfma_f32_32x32x16_bf16 v[32:47], v[88:91], v[48:51], 0\n"
    "v_exp_f32 v24, v24\n"
    "v_exp_f32 v25, v25\n"
    "s_add_u32 m0, %[wdst], 78848\n"
    "v_lshl_add_u64 v[236:237], v[236:237], 0, %[wstep]\n"
    "global_load_lds_dwordx4 v[236:237], off\n"
    "v_cvt_pk_bf16_f32 v54, v20, v21\n"
    "v_exp_f32 v26, v26\n"
    "v_exp_f32 v27, v27\n"
    "v_cvt_pk_bf16_f32 v55, v22, v23\n"
    "v_exp_f32 v28, v28\n"
    "v_exp_f32 v29, v29\n"
    "v_mfma_f32_32x32x16_bf16 v[0:15], v[140:143], v[52:55], v[0:15]\n"
    "v_cvt_pk_bf16_f32 v56, v24, v25\n"
    "v_exp_f32 v30, v30\n"
    "v_exp_f32 v31, v31\n"
    "v_cvt_pk_bf16_f32 v57, v26, v27\n"
    "v_exp_f32 v32, v32\n"
    "v_exp_f32 v33, v33\n"
    "v_cvt_pk_bf16_f32 v58, v28, v29\n"
    "s_waitcnt vmcnt(10)\n"
    "v_mfma_f32_16x16x32_bf16 v[204:207], v[176:179], v[192:195], v[204:207]\n"
    "buffer_load_dwordx4 v[176:179], v234, %[rs], %[sb] offen offset:1024 sc1\n"
    "v_exp_f32 v34, v34\n"
    "v_exp_f32 v35, v35\n"
    "v_cvt_pk_bf16_f32 v59, v30, v31\n"
    "v_exp_f32 v36, v36\n"
    "v_exp_f32 v37, v37\n"
    "v_mfma_f32_32x32x16_bf16 v[0:15], v[144:147], v[56:59], v[0:15]\n"
    "v_cvt_pk_bf16_f32 v52, v32, v33\n"
    "v_exp_f32 v38, v38\n"
    "v_exp_f32 v39, v39\n"
    "v_cvt_pk_bf16_f32 v53, v34, v35\n"
    "s_waitcnt vmcnt(9)\n"
    "v_mfma_f32_32x32x16_bf16 v[16:31], v[60:63], v[240:243], 0\n"
    "v_exp_f32 v40, v40\n"
    "v_exp_f32 v41, v41\n"
    "s_mov_b64 exec, 0xffff\n"
    "s_lshl_b64 exec, exec, 16\n"
    "buffer_load_dword v248, v247, %[rs], %[spf] offen sc1\n"
    "s_mov_b64 exec, -1\n"
    "v_cvt_pk_bf16_f32 v54, v36, v37\n"
    "v_exp_f32 v42, v42\n"
    "v_exp_f32 v43, v43\n"
    "v_cvt_pk_bf16_f32 v55, v38, v39\n"
    "v_mfma_f32_16x16x32_bf16 v[208:211], v[184:187], v[192:195], v[208:211]\n"
    "buffer_load_dwordx4 v[184:187], v234, %[rs], %[sb] offen offset:3072 sc1\n"
    "v_exp_f32 v44, v44\n"
    "v_exp_f32 v45, v45\n"
    "v_mfma_f32_32x32x16_bf16 v[0:15], v[148:151], v[52:55], v[0:15]\n"
    "v_cvt_pk_bf16_f32 v56, v40, v41\n"
    "v_exp_f32 v46, v46\n"
    "v_exp_f32 v47, v47\n"
    "v_cvt_pk_bf16_f32 v57, v42, v43\n"
    "v_exp_f32 v16, v16\n"
    "v_exp_f32 v17, v17\n"
    "v_cvt_pk_bf16_f32 v58, v44, v45\n"
    "v_exp_f32 v18, v18\n"
    "v_exp_f32 v19, v19\n"
    "v_cvt_pk_bf16_f32 v59, v46, v47\n"
    "v_exp_f32 v20, v20\n"
    "v_exp_f32 v21, v21\n"
    "v_mfma_f32_32x32x16_bf16 v[0:15], v[152:155], v[56:59], v[0:15]\n"
    "v_cvt_pk_bf16_f32 v52, v16, v17\n"
    "v_exp_f32 v22, v22\n"
    "v_exp_f32 v23, v23\n"
    "v_cvt_pk_bf16_f32 v53, v18, v19\n"
    "v_mfma_f32_32x32x16_bf16 v[32:47], v[64:67], v[240:243], 0\n"
    "v_exp_f32 v24, v24\n"
    "v_exp_f32 v25, v25\n"
    "v_cvt_pk_bf16_f32 v54, v20, v21\n"
    "v_exp_f32 v26, v26\n"
    "v_exp_f32 v27, v27\n"
    "v_cvt_pk_bf16_f32 v55, v22, v23\n"
    "v_exp_f32 v28, v28\n"
    "v_exp_f32 v29, v29\n"
    "v_mfma_f32_32x32x16_bf16 v[212:227], v[92:95], v[52:55], 0\n"
    "v_cvt_pk_bf16_f32 v56, v24, v25\n"
    "v_exp_f32 v30, v30\n"
    "ds_write2st64_b32 v228, v0, v1 offset0:0 offset1:1\n"
    "v_exp_f32 v31, v31\n"
    "v_cvt_pk_bf16_f32 v57, v26, v27\n"
    "v_exp_f32 v32, v32\n"
    "ds_write2st64_b32 v228, v2, v3 offset0:2 offset1:3\n"
    "v_exp_f32 v33, v33\n"
    "v_cvt_pk_bf16_f32 v58, v28, v29\n"
    "v_exp_f32 v34, v34\n"
    "ds_write2st64_b32 v228, v4, v5 offset0:4 offset1:5\n"
    "v_exp_f32 v35, v35\n"
    "v_cvt_pk_bf16_f32 v59, v30, v31\n"
    "v_exp_f32 v36, v36\n"
    "ds_write2st64_b32 v228, v6, v7 offset0:6 offset1:7\n"
    "v_exp_f32 v37, v37\n"
    "v_mfma_f32_32x32x16_bf16 v[212:227], v[96:99], v[56:59], v[212:227]\n"
    "v_cvt_pk_bf16_f32 v52, v32, v33\n"
    "v_exp_f32 v38, v38\n"
    "ds_write_b32 v228, v8 offset:2048\n"
    "v_exp_f32 v39, v39\n"
    "v_cvt_pk_bf16_f32 v53, v34, v35\n"
    "v_mfma_f32_32x32x16_bf16 v[16:31], v[68:71], v[240:243], 0\n"
    "v_exp_f32 v40, v40\n"
    "v_exp_f32 v41, v41\n"
    "v_cvt_pk_bf16_f32 v54, v36, v37\n"
    "v_exp_f32 v42, v42\n"
    "v_exp_f32 v43, v43\n"
    "v_cvt_pk_bf16_f32 v55, v38, v39\n"
    "v_exp_f32 v44, v44\n"
    "v_exp_f32 v45, v45\n"
    "v_mfma_f32_32x32x16_bf16 v[212:227], v[100:103], v[52:55], v[212:227]\n"
    "v_cvt_pk_bf16_f32 v56, v40, v41\n"
    "v_exp_f32 v46, v46\n"
    "v_exp_f32 v47, v47\n"
    "v_cvt_pk_bf16_f32 v57, v42, v43\n"
    "s_waitcnt lgkmcnt(0)\n"
    "s_barrier\n"
    "ds_read_b128 v[188:191], v232 offset:2048\n"
    "ds_read_b128 v[192:195], v232 offset:3072\n"
    "v_exp_f32 v16, v16\n"
    "ds_read2st64_b32 v[8:9], v230 offset0:0 offset1:9\n"
    "v_exp_f32 v17, v17\n"
    "v_cvt_pk_bf16_f32 v58, v44, v45\n"
    "v_exp_f32 v18, v18\n"
    "ds_read2st64_b32 v[10:11], v230 offset0:18 offset1:27\n"
    "v_exp_f32 v19, v19\n"
    "v_cvt_pk_bf16_f32 v59, v46, v47\n"
    "v_exp_f32 v20, v20\n"
    "ds_read2st64_b32 v[12:13], v230 offset0:36 offset1:45\n"
    "v_exp_f32 v21, v21\n"
    "v_mfma_f32_32x32x16_bf16 v[212:227], v[104:107], v[56:59], v[212:227]\n"
    "v_cvt_pk_bf16_f32 v52, v16, v17\n"
    "v_exp_f32 v22, v22\n"
    "ds_read2st64_b32 v[14:15], v230 offset0:54 offset1:63\n"
    "v_exp_f32 v23, v23\n"
    "v_cvt_pk_bf16_f32 v53, v18, v19\n"
    "v_mfma_f32_32x32x16_bf16 v[32:47], v[72:75], v[240:243], 0\n"
    "v_exp_f32 v24, v24\n"
    "ds_read2st64_b32 v[0:1], v229 offset0:0 offset1:9\n"
    "v_exp_f32 v25, v25\n"
    "s_add_u32 %[st], %[rot], 6\n"
    "s_and_b32 %[st], %[st], 7\n"
    "s_lshl_b32 %[st], %[st], 10\n"
    "s_add_u32 %[st], %[st], %[sq]\n"
    "buffer_load_dwordx4 v[48:51], v234, %[rs], %[st] offen sc1\n"
    "v_cvt_pk_bf16_f32 v54, v20, v21\n"
    "v_exp_f32 v26, v26\n"
    "ds_read2st64_b32 v[2:3], v229 offset0:18 offset1:27\n"
    "v_exp_f32 v27, v27\n"
    "v_cvt_pk_bf16_f32 v55, v22, v23\n"
    "s_waitcnt vmcnt(10)\n"
    "s_waitcnt lgkmcnt(7)\n"
    "v_mfma_f32_16x16x32_bf16 v[196:199], v[156:159], v[188:191], v[196:199]\n"
    "s_add_u32 %[sa], %[rot], 4\n"
    "s_and_b32 %[sa], %[sa], 7\n"
    "s_lshl_b32 %[sa], %[sa], 16\n"
    "s_add_u32 %[sa], %[sa], %[swo]\n"
    "buffer_load_dwordx4 v[156:159], v234, %[rs], %[sa] offen offset:0 sc1\n"
    "v_exp_f32 v28, v28\n"
    "ds_read2st64_b32 v[4:5], v229 offset0:36 offset1:45\n"
    "v_exp_f32 v29, v29\n"
    "v_mfma_f32_32x32x16_bf16 v[212:227], v[108:111], v[52:55], v[212:227]\n"
    "v_cvt_pk_bf16_f32 v56, v24, v25\n"
    "v_exp_f32 v30, v30\n"
    "ds_read2st64_b32 v[6:7], v229 offset0:54 offset1:63\n"
    "v_exp_f32 v31, v31\n"
    "v_cvt_pk_bf16_f32 v57, v26, v27\n"
    "v_exp_f32 v32, v32\n"
    "v_exp_f32 v33, v33\n"
    "s_waitcnt lgkmcnt(4)\n"
    "v_add_f32 v8, v8, v9\n"
    "v_cvt_pk_bf16_f32 v58, v28, v29\n"
    "v_exp_f32 v34, v34\n"
    "v_exp_f32 v35, v35\n"
    "v_add_f32 v10, v10, v11\n"
    "v_cvt_pk_bf16_f32 v59, v30, v31\n"
    "v_exp_f32 v36, v36\n"
    "v_exp_f32 v37, v37\n"
    "v_add_f32 v12, v12, v13\n"
    "v_mfma_f32_32x32x16_bf16 v[212:227], v[112:115], v[56:59], v[212:227]\n"
    "v_cvt_pk_bf16_f32 v52, v32, v33\n"
    "v_exp_f32 v38, v38\n"
    "v_exp_f32 v39, v39\n"
    "v_add_f32 v14, v14, v15\n"
    "v_cvt_pk_bf16_f32 v53, v34, v35\n"
    "v_mfma_f32_32x32x16_bf16 v[16:31], v[76:79], v[240:243], 0\n"
    "v_exp_f32 v40, v40\n"
    "v_exp_f32 v41, v41\n"
    "v_add_f32 v8, v8, v10\n"
    "v_cvt_pk_bf16_f32 v54, v36, v37\n"
    "v_exp_f32 v42, v42\n"
    "v_exp_f32 v43, v43\n"
    "v_add_f32 v12, v12, v14\n"
    "v_cvt_pk_bf16_f32 v55, v38, v39\n"
    "v_exp_f32 v44, v44\n"
    "v_exp_f32 v45, v45\n"
    "s_waitcnt lgkmcnt(0)\n"
    "v_add_f32 v0, v0, v1\n"
    "v_mfma_f32_32x32x16_bf16 v[212:227], v[116:119], v[52:55], v[212:227]\n"
    "v_cvt_pk_bf16_f32 v56, v40, v41\n"
    "v_exp_f32 v46, v46\n"
    "v_exp_f32 v47, v47\n"
    "v_cvt_pk_bf16_f32 v57, v42, v43\n"
    "v_exp_f32 v16, v16\n"
    "v_exp_f32 v17, v17\n"
    "v_add_f32 v8, v8, v12\n"
    "v_cvt_pk_bf16_f32 v58, v44, v45\n"
    "v_exp_f32 v18, v18\n"
    "v_exp_f32 v19, v19\n"
    "v_add_f32 v2, v2, v3\n"
    "v_cvt_pk_bf16_f32 v59, v46, v47\n"
    "v_mfma_f32_16x16x32_bf16 v[200:203], v[164:167], v[188:191], v[200:203]\n"
    "buffer_load_dwordx4 v[164:167], v234, %[rs], %[sa] offen offset:2048 sc1\n"
    "v_exp_f32 v20, v20\n"
    "v_exp_f32 v21, v21\n"
    "v_rcp_f32 v9, v8\n"
    "v_mfma_f32_32x32x16_bf16 v[212:227], v[120:123], v[56:59], v[212:227]\n"
    "v_cvt_pk_bf16_f32 v52, v16, v17\n"
    "v_exp_f32 v22, v22\n"
    "v_exp_f32 v23, v23\n"
    "v_add_f32 v4, v4, v5\n"
    "v_cvt_pk_bf16_f32 v53, v18, v19\n"
    "v_mfma_f32_32x32x16_bf16 v[32:47], v[80:83], v[240:243], 0\n"
    "v_exp_f32 v24, v24\n"
    "v_exp_f32 v25, v25\n"
    "v_add_f32 v6, v6, v7\n"
    "s_add_u32 m0, %[wdst], 86016\n"
    "v_lshl_add_u64 v[236:237], v[236:237], 0, %[wstep]\n"
    "global_load_lds_dwordx4 v[236:237], off\n"
    "v_cvt_pk_bf16_f32 v54, v20, v21\n"
    "v_exp_f32 v26, v26\n"
    "v_exp_f32 v27, v27\n"
    "v_add_f32 v0, v0, v2\n"
    "v_cvt_pk_bf16_f32 v55, v22, v23\n"
    "s_waitcnt vmcnt(10)\n"
    "v_mfma_f32_16x16x32_bf16 v[204:207], v[172:175], v[188:191], v[204:207]\n"
    "s_add_u32 %[sb], %[rot], 4\n"
    "s_and_b32 %[sb], %[sb], 7\n"
    "s_lshl_b32 %[sb], %[sb], 16\n"
    "s_add_u32 %[sb], %[sb], %[swo]\n"
    "s_add_u32 %[sb], %[sb], 4096\n"
    "buffer_load_dwordx4 v[172:175], v234, %[rs], %[sb] offen offset:0 sc1\n"
    "v_exp_f32 v28, v28\n"
    "v_exp_f32 v29, v29\n"
    "v_add_f32 v4, v4, v6\n"
    "v_mfma_f32_32x32x16_bf16 v[212:227], v[124:127], v[52:55], v[212:227]\n"
    "v_cvt_pk_bf16_f32 v56, v24, v25\n"
    "v_exp_f32 v30, v30\n"
    "v_exp_f32 v31, v31\n"
    "v_cvt_pk_bf16_f32 v57, v26, v27\n"
    "v_exp_f32 v32, v32\n"
    "v_exp_f32 v33, v33\n"
    "v_min_u32 v238, v238, v8\n"
    "v_cvt_pk_bf16_f32 v58, v28, v29\n"
    "v_mfma_f32_16x16x32_bf16 v[208:211], v[180:183], v[188:191], v[208:211]\n"
    "buffer_load_dwordx4 v[180:183], v234, %[rs], %[sb] offen offset:2048 sc1\n"
    "v_exp_f32 v34, v34\n"
    "v_exp_f32 v35, v35\n"
    "v_add_f32 v0, v0, v4\n"
    "v_cvt_pk_bf16_f32 v59, v30, v31\n"
    "v_exp_f32 v36, v36\n"
    "v_exp_f32 v37, v37\n"
    "v_add_f32 v8, |v0|, v8\n"
    "v_mfma_f32_32x32x16_bf16 v[212:227], v[128:131], v[56:59], v[212:227]\n"
    "v_cvt_pk_bf16_f32 v52, v32, v33\n"
    "v_exp_f32 v38, v38\n"
    "v_exp_f32 v39, v39\n"
    "v_mul_f32 v0, v0, v9\n"
    "v_cvt_pk_bf16_f32 v53, v34, v35\n"
    "v_mfma_f32_32x32x16_bf16 v[16:31], v[84:87], v[240:243], 0\n"
    "v_exp_f32 v40, v40\n"
    "v_exp_f32 v41, v41\n"
    "v_max_u32 v239, v239, v8\n"
    "v_cvt_pk_bf16_f32 v54, v36, v37\n"
    "v_exp_f32 v42, v42\n"
    "v_exp_f32 v43, v43\n"
    "v_cvt_pk_bf16_f32 v0, v0, v0\n"
    "v_cvt_pk_bf16_f32 v55, v38, v39\n"
    "s_waitcnt vmcnt(10)\n"
    "v_mfma_f32_16x16x32_bf16 v[196:199], v[160:163], v[192:195], v[196:199]\n"
    "buffer_load_dwordx4 v[160:163], v234, %[rs], %[sa] offen offset:1024 sc1\n"
    "v_exp_f32 v44, v44\n"
    "v_exp_f32 v45, v45\n"
    "ds_write_b16 v231, v0 offset:0\n"
    "v_mfma_f32_32x32x16_bf16 v[212:227], v[132:135], v[52:55], v[212:227]\n"
    "v_cvt_pk_bf16_f32 v56, v40, v41\n"
    "v_exp_f32 v46, v46\n"
    "v_exp_f32 v47, v47\n"
    "v_cvt_pk_bf16_f32 v57, v42, v43\n"
    "v_exp_f32 v16, v16\n"
    "v_exp_f32 v17, v17\n"
    "v_cvt_pk_bf16_f32 v58, v44, v45\n"
    "v_mfma_f32_16x16x32_bf16 v[200:203], v[168:171], v[192:195], v[200:203]\n"
    "buffer_load_dwordx4 v[168:171], v234, %[rs], %[sa] offen offset:3072 sc1\n"
    "v_exp_f32 v18, v18\n"
    "v_exp_f32 v19, v19\n"
    "v_cvt_pk_bf16_f32 v59, v46, v47\n"
    "v_exp_f32 v20, v20\n"
    "v_exp_f32 v21, v21\n"
    "v_mfma_f32_32x32x16_bf16 v[212:227], v[136:139], v[56:59], v[212:227]\n"
    "v_cvt_pk_bf16_f32 v52, v16, v17\n"
    "v_exp_f32 v22, v22\n"
    "v_exp_f32 v23, v23\n"
    "v_cvt_pk_bf16_f32 v53, v18, v19\n"
    "v_mfma_f32_32x32x16_bf16 v[32:47], v[88:91], v[240:243], 0\n"
    "v_exp_f32 v24, v24\n"
    "v_exp_f32 v25, v25\n"
    "s_add_u32 m0, %[wdst], 93184\n"
    "v_lshl_add_u64 v[236:237], v[236:237], 0, %[wstep]\n"
    "global_load_lds_dwordx4 v[236:237], off\n"
    "v_cvt_pk_bf16_f32 v54, v20, v21\n"
    "v_exp_f32 v26, v26\n"
    "v_exp_f32 v27, v27\n"
    "v_cvt_pk_bf16_f32 v55, v22, v23\n"
    "v_exp_f32 v28, v28\n"
    "v_exp_f32 v29, v29\n"
    "v_mfma_f32_32x32x16_bf16 v[212:227], v[140:143], v[52:55], v[212:227]\n"
    "v_cvt_pk_bf16_f32 v56, v24, v25\n"
    "v_exp_f32 v30, v30\n"
    "v_exp_f32 v31, v31\n"
    "v_cvt_pk_bf16_f32 v57, v26, v27\n"
    "v_exp_f32 v32, v32\n"
    "v_exp_f32 v33, v33\n"
    "v_cvt_pk_bf16_f32 v58, v28, v29\n"
    "s_waitcnt vmcnt(9)\n"
    "v_mfma_f32_16x16x32_bf16 v[204:207], v[176:179], v[192:195], v[204:207]\n"
    "buffer_load_dwordx4 v[176:179], v234, %[rs], %[sb] offen offset:1024 sc1\n"
    "v_exp_f32 v34, v34\n"
    "v_exp_f32 v35, v35\n"
    "v_cvt_pk_bf16_f32 v59, v30, v31\n"
    "v_exp_f32 v36, v36\n"
    "v_exp_f32 v37, v37\n"
    "v_mfma_f32_32x32x16_bf16 v[212:227], v[144:147], v[56:59], v[212:227]\n"
    "v_cvt_pk_bf16_f32 v52, v32, v33\n"
    "v_exp_f32 v38, v38\n"
    "v_exp_f32 v39, v39\n"
    "v_cvt_pk_bf16_f32 v53, v34, v35\n"
    "s_waitcnt vmcnt(9)\n"
    "v_mfma_f32_32x32x16_bf16 v[16:31], v[60:63], v[48:51], 0\n"
    "v_exp_f32 v40, v40\n"
    "v_exp_f32 v41, v41\n"
    "s_mov_b64 exec, 0xffff\n"
    "s_lshl_b64 exec, exec, 32\n"
    "buffer_load_dword v248, v247, %[rs], %[spf] offen sc1\n"
    "s_mov_b64 exec, -1\n"
    "v_cvt_pk_bf16_f32 v54, v36, v37\n"
    "v_exp_f32 v42, v42\n"
    "v_exp_f32 v43, v43\n"
    "v_cvt_pk_bf16_f32 v55, v38, v39\n"
    "v_mfma_f32_16x16x32_bf16 v[208:211], v[184:187], v[192:195], v[208:211]\n"
    "buffer_load_dwordx4 v[184:187], v234, %[rs], %[sb] offen offset:3072 sc1\n"
    "v_exp_f32 v44, v44\n"
    "v_exp_f32 v45, v45\n"
    "v_mfma_f32_32x32x16_bf16 v[212:227], v[148:151], v[52:55], v[212:227]\n"
    "v_cvt_pk_bf16_f32 v56, v40, v41\n"
    "v_exp_f32 v46, v46\n"
    "v_exp_f32 v47, v47\n"
    "v_cvt_pk_bf16_f32 v57, v42, v43\n"
    "v_exp_f32 v16, v16\n"
    "v_exp_f32 v17, v17\n"
    "v_cvt_pk_bf16_f32 v58, v44, v45\n"
    "v_exp_f32 v18, v18\n"
    "v_exp_f32 v19, v19\n"
    "v_lshrrev_b32 v246, 1, v234\n"
    "v_cvt_pk_bf16_f32 v59, v46, v47\n"
    "v_exp_f32 v20, v20\n"
    "v_exp_f32 v21, v21\n"
    "v_min_u32 v246, 216, v246\n"
    "v_mfma_f32_32x32x16_bf16 v[212:227], v[152:155], v[56:59], v[212:227]\n"
    "v_cvt_pk_bf16_f32 v52, v16, v17\n"
    "v_exp_f32 v22, v22\n"
    "v_exp_f32 v23, v23\n"
    "v_add_u32 v246, %[sfwd], v246\n"
    "v_cvt_pk_bf16_f32 v53, v18, v19\n"
    "v_mfma_f32_32x32x16_bf16 v[32:47], v[64:67], v[48:51], 0\n"
    "v_exp_f32 v24, v24\n"
    "v_exp_f32 v25, v25\n"
    "buffer_load_dwordx2 v[244:245], v246, %[rs], 0 offen sc1\n"
    "v_cvt_pk_bf16_f32 v54, v20, v21\n"
    "v_exp_f32 v26, v26\n"
    "v_exp_f32 v27, v27\n"
    "v_cvt_pk_bf16_f32 v55, v22, v23\n"
    "v_exp_f32 v28, v28\n"
    "v_exp_f32 v29, v29\n"
    "v_mfma_f32_32x32x16_bf16 v[0:15], v[92:95], v[52:55], 0\n"
    "v_cvt_pk_bf16_f32 v56, v24, v25\n"
    "v_exp_f32 v30, v30\n"
    "ds_write2st64_b32 v228, v212, v213 offset0:72 offset1:73\n"
    "v_exp_f32 v31, v31\n"
    "v_cvt_pk_bf16_f32 v57, v26, v27\n"
    "v_exp_f32 v32, v32\n"
    "ds_write2st64_b32 v228, v214, v215 offset0:74 offset1:75\n"
    "v_exp_f32 v33, v33\n"
    "v_cvt_pk_bf16_f32 v58, v28, v29\n"
    "v_exp_f32 v34, v34\n"
    "ds_write2st64_b32 v228, v216, v217 offset0:76 offset1:77\n"
    "v_exp_f32 v35, v35\n"
    "v_cvt_pk_bf16_f32 v59, v30, v31\n"
    "v_exp_f32 v36, v36\n"
    "ds_write2st64_b32 v228, v218, v219 offset0:78 offset1:79\n"
    "v_exp_f32 v37, v37\n"
    "v_mfma_f32_32x32x16_bf16 v[0:15], v[96:99], v[56:59], v[0:15]\n"
    "v_cvt_pk_bf16_f32 v52, v32, v33\n"
    "v_exp_f32 v38, v38\n"
    "ds_write_b32 v228, v220 offset:20480\n"
    "v_exp_f32 v39, v39\n"
    "v_cvt_pk_bf16_f32 v53, v34, v35\n"
    "v_mfma_f32_32x32x16_bf16 v[16:31], v[68:71], v[48:51], 0\n"
    "v_exp_f32 v40, v40\n"
    "v_exp_f32 v41, v41\n"
    "v_cvt_pk_bf16_f32 v54, v36, v37\n"
    "v_exp_f32 v42, v42\n"
    "v_exp_f32 v43, v43\n"
    "v_cvt_pk_bf16_f32 v55, v38, v39\n"
    "v_exp_f32 v44, v44\n"
    "v_exp_f32 v45, v45\n"
    "v_mfma_f32_32x32x16_bf16 v[0:15], v[100:103], v[52:55], v[0:15]\n"
    "v_cvt_pk_bf16_f32 v56, v40, v41\n"
    "v_exp_f32 v46, v46\n"
    "v_exp_f32 v47, v47\n"
    "v_cvt_pk_bf16_f32 v57, v42, v43\n"
    "s_waitcnt lgkmcnt(0)\n"
    "s_barrier\n"
    "ds_read_b128 v[188:191], v232 offset:0\n"
    "ds_read_b128 v[192:195], v232 offset:1024\n"
    "v_exp_f32 v16, v16\n"
    "ds_read2st64_b32 v[220:221], v230 offset0:72 offset1:81\n"
    "v_exp_f32 v17, v17\n"
    "v_cvt_pk_bf16_f32 v58, v44, v45\n"
    "v_exp_f32 v18, v18\n"
    "ds_read2st64_b32 v[222:223], v230 offset0:90 offset1:99\n"
    "v_exp_f32 v19, v19\n"
    "v_cvt_pk_bf16_f32 v59, v46, v47\n"
    "v_exp_f32 v20, v20\n"
    "ds_read2st64_b32 v[224:225], v230 offset0:108 offset1:117\n"
    "v_exp_f32 v21, v21\n"
    "v_mfma_f32_32x32x16_bf16 v[0:15], v[104:107], v[56:59], v[0:15]\n"
    "v_cvt_pk_bf16_f32 v52, v16, v17\n"
    "v_exp_f32 v22, v22\n"
    "ds_read2st64_b32 v[226:227], v230 offset0:126 offset1:135\n"
    "v_exp_f32 v23, v23\n"
    "v_cvt_pk_bf16_f32 v53, v18, v19\n"
    "v_mfma_f32_32x32x16_bf16 v[32:47], v[72:75], v[48:51], 0\n"
    "v_exp_f32 v24, v24\n"
    "ds_read2st64_b32 v[212:213], v229 offset0:72 offset1:81\n"
    "v_exp_f32 v25, v25\n"
    "s_add_u32 %[st], %[rot], 7\n"
    "s_and_b32 %[st], %[st], 7\n"
    "s_lshl_b32 %[st], %[st], 10\n"
    "s_add_u32 %[st], %[st], %[sq]\n"
    "buffer_load_dwordx4 v[240:243], v234, %[rs], %[st] offen sc1\n"
    "v_cvt_pk_bf16_f32 v54, v20, v21\n"
    "v_exp_f32 v26, v26\n"
    "ds_read2st64_b32 v[214:215], v229 offset0:90 offset1:99\n"
    "v_exp_f32 v27, v27\n"
    "v_cvt_pk_bf16_f32 v55, v22, v23\n"
    "s_waitcnt vmcnt(11)\n"
    "s_waitcnt lgkmcnt(7)\n"
    "v_mfma_f32_16x16x32_bf16 v[196:199], v[156:159], v[188:191], v[196:199]\n"
    "s_add_u32 %[sa], %[rot], 5\n"
    "s_and_b32 %[sa], %[sa], 7\n"
    "s_lshl_b32 %[sa], %[sa], 16\n"
    "s_add_u32 %[sa], %[sa], %[swo]\n"
    "buffer_load_dwordx4 v[156:159], v234, %[rs], %[sa] offen offset:0 sc1\n"
    "v_exp_f32 v28, v28\n"
    "ds_read2st64_b32 v[216:217], v229 offset0:108 offset1:117\n"
    "v_exp_f32 v29, v29\n"
    "v_mfma_f32_32x32x16_bf16 v[0:15], v[108:111], v[52:55], v[0:15]\n"
    "v_cvt_pk_bf16_f32 v56, v24, v25\n"
    "v_exp_f32 v30, v30\n"
    "ds_read2st64_b32 v[218:219], v229 offset0:126 offset1:135\n"
    "v_exp_f32 v31, v31\n"
    "v_cvt_pk_bf16_f32 v57, v26, v27\n"
    "v_exp_f32 v32, v32\n"
    "v_exp_f32 v33, v33\n"
    "s_waitcnt lgkmcnt(4)\n"
    "v_add_f32 v220, v220, v221\n"
    "v_cvt_pk_bf16_f32 v58, v28, v29\n"
    "v_exp_f32 v34, v34\n"
    "v_exp_f32 v35, v35\n"
    "v_add_f32 v222, v222, v223\n"
    "v_cvt_pk_bf16_f32 v59, v30, v31\n"
    "v_exp_f32 v36, v36\n"
    "v_exp_f32 v37, v37\n"
    "v_add_f32 v224, v224, v225\n"
    "v_mfma_f32_32x32x16_bf16 v[0:15], v[112:115], v[56:59], v[0:15]\n"
    "v_cvt_pk_bf16_f32 v52, v32, v33\n"
    "v_exp_f32 v38, v38\n"
    "v_exp_f32 v39, v39\n"
    "v_add_f32 v226, v226, v227\n"
    "v_cvt_pk_bf16_f32 v53, v34, v35\n"
    "v_mfma_f32_32x32x16_bf16 v[16:31], v[76:79], v[48:51], 0\n"
    "v_exp_f32 v40, v40\n"
    "v_exp_f32 v41, v41\n"
    "v_add_f32 v220, v220, v222\n"
    "v_cvt_pk_bf16_f32 v54, v36, v37\n"
    "v_exp_f32 v42, v42\n"
    "v_exp_f32 v43, v43\n"
    "v_add_f32 v224, v224, v226\n"
    "v_cvt_pk_bf16_f32 v55, v38, v39\n"
    "v_exp_f32 v44, v44\n"
    "v_exp_f32 v45, v45\n"
    "s_waitcnt lgkmcnt(0)\n"
    "v_add_f32 v212, v212, v213\n"
    "v_mfma_f32_32x32x16_bf16 v[0:15], v[116:119], v[52:55], v[0:15]\n"
    "v_cvt_pk_bf16_f32 v56, v40, v41\n"
    "v_exp_f32 v46, v46\n"
    "v_exp_f32 v47, v47\n"
    "v_cvt_pk_bf16_f32 v57, v42, v43\n"
    "v_exp_f32 v16, v16\n"
    "v_exp_f32 v17, v17\n"
    "v_add_f32 v220, v220, v224\n"
    "v_cvt_pk_bf16_f32 v58, v44, v45\n"
    "v_exp_f32 v18, v18\n"
    "v_exp_f32 v19, v19\n"
    "v_add_f32 v214, v214, v215\n"
    "v_cvt_pk_bf16_f32 v59, v46, v47\n"
    "v_mfma_f32_16x16x32_bf16 v[200:203], v[164:167], v[188:191], v[200:203]\n"
    "buffer_load_dwordx4 v[164:167], v234, %[rs], %[sa] offen offset:2048 sc1\n"
    "v_exp_f32 v20, v20\n"
    "v_exp_f32 v21, v21\n"
    "v_rcp_f32 v221, v220\n"
    "v_mfma_f32_32x32x16_bf16 v[0:15], v[120:123], v[56:59], v[0:15]\n"
    "v_cvt_pk_bf16_f32 v52, v16, v17\n"
    "v_exp_f32 v22, v22\n"
    "v_exp_f32 v23, v23\n"
    "v_add_f32 v216, v216, v217\n"
    "v_cvt_pk_bf16_f32 v53, v18, v19\n"
    "v_mfma_f32_32x32x16_bf16 v[32:47], v[80:83], v[48:51], 0\n"
    "v_exp_f32 v24, v24\n"
    "v_exp_f32 v25, v25\n"
    "v_add_f32 v218, v218, v219\n"
    "s_add_u32 m0, %[wdst], 100352\n"
    "v_lshl_add_u64 v[236:237], v[236:237], 0, %[wstep]\n"
    "global_load_lds_dwordx4 v[236:237], off\n"
    "v_cvt_pk_bf16_f32 v54, v20, v21\n"
    "v_exp_f32 v26, v26\n"
    "v_exp_f32 v27, v27\n"
    "v_add_f32 v212, v212, v214\n"
    "v_cvt_pk_bf16_f32 v55, v22, v23\n"
    "s_waitcnt vmcnt(11)\n"
    "v_mfma_f32_16x16x32_bf16 v[204:207], v[172:175], v[188:191], v[204:207]\n"
    "s_add_u32 %[sb], %[rot], 5\n"
    "s_and_b32 %[sb], %[sb], 7\n"
    "s_lshl_b32 %[sb], %[sb], 16\n"
    "s_add_u32 %[sb], %[sb], %[swo]\n"
    "s_add_u32 %[sb], %[sb], 4096\n"
    "buffer_load_dwordx4 v[172:175], v234, %[rs], %[sb] offen offset:0 sc1\n"
    "v_exp_f32 v28, v28\n"
    "v_exp_f32 v29, v29\n"
    "v_add_f32 v216, v216, v218\n"
    "v_mfma_f32_32x32x16_bf16 v[0:15], v[124:127], v[52:55], v[0:15]\n"
    "v_cvt_pk_bf16_f32 v56, v24, v25\n"
    "v_exp_f32 v30, v30\n"
    "v_exp_f32 v31, v31\n"
    "v_cvt_pk_bf16_f32 v57, v26, v27\n"
    "v_exp_f32 v32, v32\n"
    "v_exp_f32 v33, v33\n"
    "v_min_u32 v238, v238, v220\n"
    "v_cvt_pk_bf16_f32 v58, v28, v29\n"
    "v_mfma_f32_16x16x32_bf16 v[208:211], v[180:183], v[188:191], v[208:211]\n"
    "buffer_load_dwordx4 v[180:183], v234, %[rs], %[sb] offen offset:2048 sc1\n"
    "v_exp_f32 v34, v34\n"
    "v_exp_f32 v35, v35\n"
    "v_add_f32 v212, v212, v216\n"
    "v_cvt_pk_bf16_f32 v59, v30, v31\n"
    "v_exp_f32 v36, v36\n"
    "v_exp_f32 v37, v37\n"
    "v_add_f32 v220, |v212|, v220\n"
    "v_mfma_f32_32x32x16_bf16 v[0:15], v[128:131], v[56:59], v[0:15]\n"
    "v_cvt_pk_bf16_f32 v52, v32, v33\n"
    "v_exp_f32 v38, v38\n"
    "v_exp_f32 v39, v39\n"
    "v_mul_f32 v212, v212, v221\n"
    "v_cvt_pk_bf16_f32 v53, v34, v35\n"
    "v_mfma_f32_32x32x16_bf16 v[16:31], v[84:87], v[48:51], 0\n"
    "v_exp_f32 v40, v40\n"
    "v_exp_f32 v41, v41\n"
    "v_max_u32 v239, v239, v220\n"
    "v_cvt_pk_bf16_f32 v54, v36, v37\n"
    "v_exp_f32 v42, v42\n"
    "v_exp_f32 v43, v43\n"
    "v_cvt_pk_bf16_f32 v212, v212, v212\n"
    "v_cvt_pk_bf16_f32 v55, v38, v39\n"
    "s_waitcnt vmcnt(11)\n"
    "v_mfma_f32_16x16x32_bf16 v[196:199], v[160:163], v[192:195], v[196:199]\n"
    "buffer_load_dwordx4 v[160:163], v234, %[rs], %[sa] offen offset:1024 sc1\n"
    "v_exp_f32 v44, v44\n"
    "v_exp_f32 v45, v45\n"
    "ds_write_b16 v231, v212 offset:2048\n"
    "v_mfma_f32_32x32x16_bf16 v[0:15], v[132:135], v[52:55], v[0:15]\n"
    "v_cvt_pk_bf16_f32 v56, v40, v41\n"
    "v_exp_f32 v46, v46\n"
    "v_exp_f32 v47, v47\n"
    "v_cvt_pk_bf16_f32 v57, v42, v43\n"
    "v_exp_f32 v16, v16\n"
    "v_exp_f32 v17, v17\n"
    "v_cvt_pk_bf16_f32 v58, v44, v45\n"
    "v_mfma_f32_16x16x32_bf16 v[200:203], v[168:171], v[192:195], v[200:203]\n"
    "buffer_load_dwordx4 v[168:171], v234, %[rs], %[sa] offen offset:3072 sc1\n"
    "v_exp_f32 v18, v18\n"
    "v_exp_f32 v19, v19\n"
    "v_cvt_pk_bf16_f32 v59, v46, v47\n"
    "v_exp_f32 v20, v20\n"
    "v_exp_f32 v21, v21\n"
    "v_mfma_f32_32x32x16_bf16 v[0:15], v[136:139], v[56:59], v[0:15]\n"
    "v_cvt_pk_bf16_f32 v52, v16, v17\n"
    "v_exp_f32 v22, v22\n"
    "v_exp_f32 v23, v23\n"
    "v_cvt_pk_bf16_f32 v53, v18, v19\n"
    "v_mfma_f32_32x32x16_bf16 v[32:47], v[88:91], v[48:51], 0\n"
    "v_exp_f32 v24, v24\n"
    "v_exp_f32 v25, v25\n"
    "s_add_u32 m0, %[wdst], 107520\n"
    "v_lshl_add_u64 v[236:237], v[236:237], 0, %[wstep]\n"
    "global_load_lds_dwordx4 v[236:237], off\n"
    "v_cvt_pk_bf16_f32 v54, v20, v21\n"
    "v_exp_f32 v26, v26\n"
    "v_exp_f32 v27, v27\n"
    "v_cvt_pk_bf16_f32 v55, v22, v23\n"
    "v_exp_f32 v28, v28\n"
    "v_exp_f32 v29, v29\n"
    "v_mfma_f32_32x32x16_bf16 v[0:15], v[140:143], v[52:55], v[0:15]\n"
    "v_cvt_pk_bf16_f32 v56, v24, v25\n"
    "v_exp_f32 v30, v30\n"
    "v_exp_f32 v31, v31\n"
    "v_cvt_pk_bf16_f32 v57, v26, v27\n"
    "v_exp_f32 v32, v32\n"
    "v_exp_f32 v33, v33\n"
    "v_cvt_pk_bf16_f32 v58, v28, v29\n"
    "s_waitcnt vmcnt(10)\n"
    "v_mfma_f32_16x16x32_bf16 v[204:207], v[176:179], v[192:195], v[204:207]\n"
    "buffer_load_dwordx4 v[176:179], v234, %[rs], %[sb] offen offset:1024 sc1\n"
    "v_exp_f32 v34, v34\n"
    "v_exp_f32 v35, v35\n"
    "v_cvt_pk_bf16_f32 v59, v30, v31\n"
    "v_exp_f32 v36, v36\n"
    "v_exp_f32 v37, v37\n"
    "v_mfma_f32_32x32x16_bf16 v[0:15], v[144:147], v[56:59], v[0:15]\n"
    "v_cvt_pk_bf16_f32 v52, v32, v33\n"
    "v_exp_f32 v38, v38\n"
    "v_exp_f32 v39, v39\n"
    "v_cvt_pk_bf16_f32 v53, v34, v35\n"
    "s_waitcnt vmcnt(9)\n"
    "v_mfma_f32_32x32x16_bf16 v[16:31], v[60:63], v[240:243], 0\n"
    "v_exp_f32 v40, v40\n"
    "v_exp_f32 v41, v41\n"
    "s_mov_b64 exec, 0xffff\n"
    "s_lshl_b64 exec, exec, 48\n"
    "buffer_load_dword v248, v247, %[rs], %[spf] offen sc1\n"
    "s_mov_b64 exec, -1\n"
    "v_cvt_pk_bf16_f32 v54, v36, v37\n"
    "v_exp_f32 v42, v42\n"
    "v_exp_f32 v43, v43\n"
    "v_cvt_pk_bf16_f32 v55, v38, v39\n"
    "v_mfma_f32_16x16x32_bf16 v[208:211], v[184:187], v[192:195], v[208:211]\n"
    "buffer_load_dwordx4 v[184:187], v234, %[rs], %[sb] offen offset:3072 sc1\n"
    "v_exp_f32 v44, v44\n"
    "v_exp_f32 v45, v45\n"
    "v_mfma_f32_32x32x16_bf16 v[0:15], v[148:151], v[52:55], v[0:15]\n"
    "v_cvt_pk_bf16_f32 v56, v40, v41\n"
    "v_exp_f32 v46, v46\n"
    "v_exp_f32 v47, v47\n"
    "v_cvt_pk_bf16_f32 v57, v42, v43\n"
    "v_exp_f32 v16, v16\n"
    "v_exp_f32 v17, v17\n"
    "s_add_u32 %[sa], %[rot], 6\n"
    "s_and_b32 %[sa], %[sa], 7\n"
    "s_lshl_b32 %[sa], %[sa], 16\n"
    "s_add_u32 %[sa], %[sa], %[swo]\n"
    "buffer_load_dwordx4 v[60:63], v234, %[rs], %[sa] offen offset:0 sc1\n"
    "v_cvt_pk_bf16_f32 v58, v44, v45\n"
    "v_exp_f32 v18, v18\n"
    "v_exp_f32 v19, v19\n"
    "v_cvt_pk_bf16_f32 v59, v46, v47\n"
    "v_exp_f32 v20, v20\n"
    "v_exp_f32 v21, v21\n"
    "v_mfma_f32_32x32x16_bf16 v[0:15], v[152:155], v[56:59], v[0:15]\n"
    "v_cvt_pk_bf16_f32 v52, v16, v17\n"
    "v_exp_f32 v22, v22\n"
    "v_exp_f32 v23, v23\n"
    "v_cvt_pk_bf16_f32 v53, v18, v19\n"
    "v_mfma_f32_32x32x16_bf16 v[32:47], v[64:67], v[240:243], 0\n"
    "v_exp_f32 v24, v24\n"
    "v_exp_f32 v25, v25\n"
    "v_cvt_pk_bf16_f32 v54, v20, v21\n"
    "v_exp_f32 v26, v26\n"
    "v_exp_f32 v27, v27\n"
    "v_cvt_pk_bf16_f32 v55, v22, v23\n"
    "buffer_load_dwordx4 v[64:67], v234, %[rs], %[sa] offen offset:2048 sc1\n"
    "v_exp_f32 v28, v28\n"
    "v_exp_f32 v29, v29\n"
    "v_mfma_f32_32x32x16_bf16 v[212:227], v[92:95], v[52:55], 0\n"
    "v_cvt_pk_bf16_f32 v56, v24, v25\n"
    "v_exp_f32 v30, v30\n"
    "ds_write2st64_b32 v228, v0, v1 offset0:0 offset1:1\n"
    "v_exp_f32 v31, v31\n"
    "s_add_u32 %[sb], %[rot], 6\n"
    "s_and_b32 %[sb], %[sb], 7\n"
    "s_lshl_b32 %[sb], %[sb], 16\n"
    "s_add_u32 %[sb], %[sb], %[swo]\n"
    "s_add_u32 %[sb], %[sb], 4096\n"
    "buffer_load_dwordx4 v[92:95], v234, %[rs], %[sb] offen offset:0 sc1\n"
    "v_cvt_pk_bf16_f32 v57, v26, v27\n"
    "v_exp_f32 v32, v32\n"
    "ds_write2st64_b32 v228, v2, v3 offset0:2 offset1:3\n"
    "v_exp_f32 v33, v33\n"
    "v_cvt_pk_bf16_f32 v58, v28, v29\n"
    "v_exp_f32 v34, v34\n"
    "ds_write2st64_b32 v228, v4, v5 offset0:4 offset1:5\n"
    "v_exp_f32 v35, v35\n"
    "v_cvt_pk_bf16_f32 v59, v30, v31\n"
    "v_exp_f32 v36, v36\n"
    "ds_write2st64_b32 v228, v6, v7 offset0:6 offset1:7\n"
    "v_exp_f32 v37, v37\n"
    "v_mfma_f32_32x32x16_bf16 v[212:227], v[96:99], v[56:59], v[212:227]\n"
    "v_cvt_pk_bf16_f32 v52, v32, v33\n"
    "v_exp_f32 v38, v38\n"
    "ds_write_b32 v228, v8 offset:2048\n"
    "v_exp_f32 v39, v39\n"
    "v_cvt_pk_bf16_f32 v53, v34, v35\n"
    "v_mfma_f32_32x32x16_bf16 v[16:31], v[68:71], v[240:243], 0\n"
    "v_exp_f32 v40, v40\n"
    "v_exp_f32 v41, v41\n"
    "v_cvt_pk_bf16_f32 v54, v36, v37\n"
    "v_exp_f32 v42, v42\n"
    "v_exp_f32 v43, v43\n"
    "v_cvt_pk_bf16_f32 v55, v38, v39\n"
    "buffer_load_dwordx4 v[68:71], v234, %[rs], %[sb] offen offset:2048 sc1\n"
    "buffer_load_dwordx4 v[96:99], v234, %[rs], %[sa] offen offset:1024 sc1\n"
    "v_exp_f32 v44, v44\n"
    "v_exp_f32 v45, v45\n"
    "v_mfma_f32_32x32x16_bf16 v[212:227], v[100:103], v[52:55], v[212:227]\n"
    "v_cvt_pk_bf16_f32 v56, v40, v41\n"
    "v_exp_f32 v46, v46\n"
    "v_exp_f32 v47, v47\n"
    "buffer_load_dwordx4 v[100:103], v234, %[rs], %[sa] offen offset:3072 sc1\n"
    "v_cvt_pk_bf16_f32 v57, v42, v43\n"
    "s_waitcnt lgkmcnt(0)\n"
    "s_barrier\n"
    "ds_read_b128 v[188:191], v232 offset:2048\n"
    "ds_read_b128 v[192:195], v232 offset:3072\n"
    "v_exp_f32 v16, v16\n"
    "ds_read2st64_b32 v[8:9], v230 offset0:0 offset1:9\n"
    "v_exp_f32 v17, v17\n"
    "v_cvt_pk_bf16_f32 v58, v44, v45\n"
    "s_waitcnt vmcnt(16)\n"
    "s_waitcnt lgkmcnt(2)\n"
    "v_mfma_f32_16x16x32_bf16 v[196:199], v[156:159], v[188:191], v[196:199]\n"
    "v_exp_f32 v18, v18\n"
    "ds_read2st64_b32 v[10:11], v230 offset0:18 offset1:27\n"
    "v_exp_f32 v19, v19\n"
    "v_cvt_pk_bf16_f32 v59, v46, v47\n"
    "v_exp_f32 v20, v20\n"
    "ds_read2st64_b32 v[12:13], v230 offset0:36 offset1:45\n"
    "v_exp_f32 v21, v21\n"
    "v_mfma_f32_32x32x16_bf16 v[212:227], v[104:107], v[56:59], v[212:227]\n"
    "v_cvt_pk_bf16_f32 v52, v16, v17\n"
    "v_exp_f32 v22, v22\n"
    "ds_read2st64_b32 v[14:15], v230 offset0:54 offset1:63\n"
    "v_exp_f32 v23, v23\n"
    "v_cvt_pk_bf16_f32 v53, v18, v19\n"
    "v_mfma_f32_32x32x16_bf16 v[32:47], v[72:75], v[240:243], 0\n"
    "v_exp_f32 v24, v24\n"
    "ds_read2st64_b32 v[0:1], v229 offset0:0 offset1:9\n"
    "v_exp_f32 v25, v25\n"
    "v_cvt_pk_bf16_f32 v54, v20, v21\n"
    "v_exp_f32 v26, v26\n"
    "ds_read2st64_b32 v[2:3], v229 offset0:18 offset1:27\n"
    "buffer_load_dwordx4 v[72:75], v234, %[rs], %[sb] offen offset:1024 sc1\n"
    "buffer_load_dwordx4 v[104:107], v234, %[rs], %[sb] offen offset:3072 sc1\n"
    "v_exp_f32 v27, v27\n"
    "v_cvt_pk_bf16_f32 v55, v22, v23\n"
    "s_waitcnt vmcnt(17)\n"
    "v_mfma_f32_16x16x32_bf16 v[200:203], v[164:167], v[188:191], v[200:203]\n"
    "v_exp_f32 v28, v28\n"
    "ds_read2st64_b32 v[4:5], v229 offset0:36 offset1:45\n"
    "v_exp_f32 v29, v29\n"
    "v_mfma_f32_32x32x16_bf16 v[212:227], v[108:111], v[52:55], v[212:227]\n"
    "v_cvt_pk_bf16_f32 v56, v24, v25\n"
    "v_exp_f32 v30, v30\n"
    "ds_read2st64_b32 v[6:7], v229 offset0:54 offset1:63\n"
    "v_exp_f32 v31, v31\n"
    "v_cvt_pk_bf16_f32 v57, v26, v27\n"
    "v_exp_f32 v32, v32\n"
    "s_waitcnt lgkmcnt(4)\n"
    "v_add_f32 v8, v8, v9\n"
    "s_add_u32 %[sa], %[rot], 7\n"
    "s_and_b32 %[sa], %[sa], 7\n"
    "s_lshl_b32 %[sa], %[sa], 16\n"
    "s_add_u32 %[sa], %[sa], %[swo]\n"
    "buffer_load_dwordx4 v[108:111], v234, %[rs], %[sa] offen offset:0 sc1\n"
    "v_exp_f32 v33, v33\n"
    "v_add_f32 v10, v10, v11\n"
    "v_cvt_pk_bf16_f32 v58, v28, v29\n"
    "s_waitcnt vmcnt(16)\n"
    "v_mfma_f32_16x16x32_bf16 v[204:207], v[172:175], v[188:191], v[204:207]\n"
    "v_exp_f32 v34, v34\n"
    "v_add_f32 v12, v12, v13\n"
    "v_exp_f32 v35, v35\n"
    "v_add_f32 v14, v14, v15\n"
    "v_cvt_pk_bf16_f32 v59, v30, v31\n"
    "s_waitcnt vmcnt(15)\n"
    "v_mfma_f32_16x16x32_bf16 v[208:211], v[180:183], v[188:191], v[208:211]\n"
    "v_exp_f32 v36, v36\n"
    "v_add_f32 v8, v8, v10\n"
    "v_exp_f32 v37, v37\n"
    "v_add_f32 v12, v12, v14\n"
    "v_mfma_f32_32x32x16_bf16 v[212:227], v[112:115], v[56:59], v[212:227]\n"
    "v_cvt_pk_bf16_f32 v52, v32, v33\n"
    "v_exp_f32 v38, v38\n"
    "s_waitcnt lgkmcnt(0)\n"
    "v_add_f32 v0, v0, v1\n"
    "v_exp_f32 v39, v39\n"
    "v_add_f32 v8, v8, v12\n"
    "v_cvt_pk_bf16_f32 v53, v34, v35\n"
    "v_mfma_f32_32x32x16_bf16 v[16:31], v[76:79], v[240:243], 0\n"
    "v_exp_f32 v40, v40\n"
    "v_add_f32 v2, v2, v3\n"
    "v_exp_f32 v41, v41\n"
    "v_rcp_f32 v9, v8\n"
    "v_cvt_pk_bf16_f32 v54, v36, v37\n"
    "v_exp_f32 v42, v42\n"
    "v_add_f32 v4, v4, v5\n"
    "buffer_load_dwordx4 v[76:79], v234, %[rs], %[sa] offen offset:2048 sc1\n"
    "s_add_u32 %[sb], %[rot], 7\n"
    "s_and_b32 %[sb], %[sb], 7\n"
    "s_lshl_b32 %[sb], %[sb], 16\n"
    "s_add_u32 %[sb], %[sb], %[swo]\n"
    "s_add_u32 %[sb], %[sb], 4096\n"
    "buffer_load_dwordx4 v[112:115], v234, %[rs], %[sb] offen offset:0 sc1\n"
    "v_exp_f32 v43, v43\n"
    "v_add_f32 v6, v6, v7\n"
    "v_cvt_pk_bf16_f32 v55, v38, v39\n"
    "s_waitcnt vmcnt(16)\n"
    "v_mfma_f32_16x16x32_bf16 v[196:199], v[160:163], v[192:195], v[196:199]\n"
    "v_exp_f32 v44, v44\n"
    "v_add_f32 v0, v0, v2\n"
    "v_exp_f32 v45, v45\n"
    "v_add_f32 v4, v4, v6\n"
    "v_mfma_f32_32x32x16_bf16 v[212:227], v[116:119], v[52:55], v[212:227]\n"
    "v_cvt_pk_bf16_f32 v56, v40, v41\n"
    "v_exp_f32 v46, v46\n"
    "v_min_u32 v238, v238, v8\n"
    "v_exp_f32 v47, v47\n"
    "v_add_f32 v0, v0, v4\n"
    "v_cvt_pk_bf16_f32 v57, v42, v43\n"
    "v_exp_f32 v16, v16\n"
    "v_add_f32 v8, |v0|, v8\n"
    "buffer_load_dwordx4 v[116:119], v234, %[rs], %[sb] offen offset:2048 sc1\n"
    "v_exp_f32 v17, v17\n"
    "v_mul_f32 v0, v0, v9\n"
    "v_cvt_pk_bf16_f32 v58, v44, v45\n"
    "s_waitcnt vmcnt(16)\n"
    "v_mfma_f32_16x16x32_bf16 v[200:203], v[168:171], v[192:195], v[200:203]\n"
    "v_exp_f32 v18, v18\n"
    "v_max_u32 v239, v239, v8\n"
    "v_exp_f32 v19, v19\n"
    "v_cvt_pk_bf16_f32 v0, v0, v0\n"
    "v_cvt_pk_bf16_f32 v59, v46, v47\n"
    "s_waitcnt vmcnt(14)\n"
    "v_mfma_f32_16x16x32_bf16 v[204:207], v[176:179], v[192:195], v[204:207]\n"
    "v_exp_f32 v20, v20\n"
    "ds_write_b16 v231, v0 offset:0\n"
    "v_exp_f32 v21, v21\n"
    "v_mfma_f32_32x32x16_bf16 v[212:227], v[120:123], v[56:59], v[212:227]\n"
    "v_cvt_pk_bf16_f32 v52, v16, v17\n"
    "v_exp_f32 v22, v22\n"
    "v_exp_f32 v23, v23\n"
    "v_cvt_pk_bf16_f32 v53, v18, v19\n"
    "v_mfma_f32_32x32x16_bf16 v[32:47], v[80:83], v[240:243], 0\n"
    "v_exp_f32 v24, v24\n"
    "v_exp_f32 v25, v25\n"
    "v_cvt_pk_bf16_f32 v54, v20, v21\n"
    "v_exp_f32 v26, v26\n"
    "buffer_load_dwordx4 v[80:83], v234, %[rs], %[sa] offen offset:1024 sc1\n"
    "buffer_load_dwordx4 v[120:123], v234, %[rs], %[sa] offen offset:3072 sc1\n"
    "v_exp_f32 v27, v27\n"
    "v_cvt_pk_bf16_f32 v55, v22, v23\n"
    "s_waitcnt vmcnt(14)\n"
    "v_mfma_f32_16x16x32_bf16 v[208:211], v[184:187], v[192:195], v[208:211]\n"
    "v_exp_f32 v28, v28\n"
    "v_exp_f32 v29, v29\n"
    "v_mfma_f32_32x32x16_bf16 v[212:227], v[124:127], v[52:55], v[212:227]\n"
    "v_cvt_pk_bf16_f32 v56, v24, v25\n"
    "v_exp_f32 v30, v30\n"
    "v_exp_f32 v31, v31\n"
    "v_cvt_pk_bf16_f32 v57, v26, v27\n"
    "s_waitcnt lgkmcnt(0)\n"
    "s_barrier\n"
    "ds_read_b128 v[188:191], v232 offset:0\n"
    "ds_read_b128 v[192:195], v232 offset:1024\n"
    "v_exp_f32 v32, v32\n"
    "buffer_load_dwordx4 v[124:127], v234, %[rs], %[sb] offen offset:1024 sc1\n"
    "v_exp_f32 v33, v33\n"
    "v_cvt_pk_bf16_f32 v58, v28, v29\n"
    "v_exp_f32 v34, v34\n"
    "v_exp_f32 v35, v35\n"
    "v_cvt_pk_bf16_f32 v59, v30, v31\n"
    "v_exp_f32 v36, v36\n"
    "v_exp_f32 v37, v37\n"
    "v_mfma_f32_32x32x16_bf16 v[212:227], v[128:131], v[56:59], v[212:227]\n"
    "v_cvt_pk_bf16_f32 v52, v32, v33\n"
    "v_exp_f32 v38, v38\n"
    "v_exp_f32 v39, v39\n"
    "v_cvt_pk_bf16_f32 v53, v34, v35\n"
    "v_mfma_f32_32x32x16_bf16 v[16:31], v[84:87], v[240:243], 0\n"
    "v_exp_f32 v40, v40\n"
    "v_exp_f32 v41, v41\n"
    "v_cvt_pk_bf16_f32 v54, v36, v37\n"
    "v_exp_f32 v42, v42\n"
    "buffer_load_dwordx4 v[84:87], v234, %[rs], %[sb] offen offset:3072 sc1\n"
    "v_exp_f32 v43, v43\n"
    "v_cvt_pk_bf16_f32 v55, v38, v39\n"
    "s_waitcnt vmcnt(15)\n"
    "s_waitcnt lgkmcnt(1)\n"
    "v_mfma_f32_16x16x32_bf16 v[196:199], v[60:63], v[188:191], v[196:199]\n"
    "v_exp_f32 v44, v44\n"
    "v_exp_f32 v45, v45\n"
    "v_mfma_f32_32x32x16_bf16 v[212:227], v[132:135], v[52:55], v[212:227]\n"
    "v_cvt_pk_bf16_f32 v56, v40, v41\n"
    "v_exp_f32 v46, v46\n"
    "v_exp_f32 v47, v47\n"
    "v_cvt_pk_bf16_f32 v57, v42, v43\n"
    "v_exp_f32 v16, v16\n"
    "v_exp_f32 v17, v17\n"
    "v_cvt_pk_bf16_f32 v58, v44, v45\n"
    "s_waitcnt vmcnt(14)\n"
    "v_mfma_f32_16x16x32_bf16 v[200:203], v[64:67], v[188:191], v[200:203]\n"
    "v_exp_f32 v18, v18\n"
    "v_exp_f32 v19, v19\n"
    "v_cvt_pk_bf16_f32 v59, v46, v47\n"
    "s_waitcnt vmcnt(13)\n"
    "v_mfma_f32_16x16x32_bf16 v[204:207], v[92:95], v[188:191], v[204:207]\n"
    "v_exp_f32 v20, v20\n"
    "v_exp_f32 v21, v21\n"
    "v_mfma_f32_32x32x16_bf16 v[212:227], v[136:139], v[56:59], v[212:227]\n"
    "v_cvt_pk_bf16_f32 v52, v16, v17\n"
    "v_exp_f32 v22, v22\n"
    "v_exp_f32 v23, v23\n"
    "v_cvt_pk_bf16_f32 v53, v18, v19\n"
    "v_mfma_f32_32x32x16_bf16 v[32:47], v[88:91], v[240:243], 0\n"
    "v_exp_f32 v24, v24\n"
    "v_exp_f32 v25, v25\n"
    "v_cvt_pk_bf16_f32 v54, v20, v21\n"
    "v_exp_f32 v26, v26\n"
    "global_load_dwordx2 v[88:89], v252, %[xb]\n"
    "v_exp_f32 v27, v27\n"
    "global_load_dwordx2 v[90:91], v252, %[xb] offset:32\n"
    "v_cvt_pk_bf16_f32 v55, v22, v23\n"
    "s_waitcnt vmcnt(14)\n"
    "v_mfma_f32_16x16x32_bf16 v[208:211], v[68:71], v[188:191], v[208:211]\n"
    "v_exp_f32 v28, v28\n"
    "v_exp_f32 v29, v29\n"
    "v_mfma_f32_32x32x16_bf16 v[212:227], v[140:143], v[52:55], v[212:227]\n"
    "v_cvt_pk_bf16_f32 v56, v24, v25\n"
    "v_exp_f32 v30, v30\n"
    "s_add_u32 %[st], %[swd], 0\n"
    "buffer_load_dwordx4 v[156:159], v234, %[rs], %[st] offen offset:0 sc1\n"
    "v_exp_f32 v31, v31\n"
    "v_cvt_pk_bf16_f32 v57, v26, v27\n"
    "v_exp_f32 v32, v32\n"
    "v_exp_f32 v33, v33\n"
    "v_cvt_pk_bf16_f32 v58, v28, v29\n"
    "s_waitcnt vmcnt(14)\n"
    "s_waitcnt lgkmcnt(0)\n"
    "v_mfma_f32_16x16x32_bf16 v[196:199], v[96:99], v[192:195], v[196:199]\n"
    "v_exp_f32 v34, v34\n"
    "s_add_u32 %[st], %[swd], 0\n"
    "buffer_load_dwordx4 v[160:163], v234, %[rs], %[st] offen offset:1024 sc1\n"
    "v_exp_f32 v35, v35\n"
    "v_cvt_pk_bf16_f32 v59, v30, v31\n"
    "s_waitcnt vmcnt(14)\n"
    "v_mfma_f32_16x16x32_bf16 v[200:203], v[100:103], v[192:195], v[200:203]\n"
    "v_exp_f32 v36, v36\n"
    "v_exp_f32 v37, v37\n"
    "v_mfma_f32_32x32x16_bf16 v[212:227], v[144:147], v[56:59], v[212:227]\n"
    "v_cvt_pk_bf16_f32 v52, v32, v33\n"
    "v_exp_f32 v38, v38\n"
    "v_exp_f32 v39, v39\n"
    "v_cvt_pk_bf16_f32 v53, v34, v35\n"
    "v_exp_f32 v40, v40\n"
    "s_add_u32 %[st], %[swd], 0\n"
    "buffer_load_dwordx4 v[164:167], v234, %[rs], %[st] offen offset:2048 sc1\n"
    "v_exp_f32 v41, v41\n"
    "v_cvt_pk_bf16_f32 v54, v36, v37\n"
    "s_waitcnt vmcnt(14)\n"
    "v_mfma_f32_16x16x32_bf16 v[204:207], v[72:75], v[192:195], v[204:207]\n"
    "v_exp_f32 v42, v42\n"
    "v_exp_f32 v43, v43\n"
    "v_cvt_pk_bf16_f32 v55, v38, v39\n"
    "s_waitcnt vmcnt(13)\n"
    "v_mfma_f32_16x16x32_bf16 v[208:211], v[104:107], v[192:195], v[208:211]\n"
    "v_exp_f32 v44, v44\n"
    "v_exp_f32 v45, v45\n"
    "v_mfma_f32_32x32x16_bf16 v[212:227], v[148:151], v[52:55], v[212:227]\n"
    "v_cvt_pk_bf16_f32 v56, v40, v41\n"
    "v_exp_f32 v46, v46\n"
    "s_add_u32 %[st], %[swd], 0\n"
    "buffer_load_dwordx4 v[168:171], v234, %[rs], %[st] offen offset:3072 sc1\n"
    "v_exp_f32 v47, v47\n"
    "v_cvt_pk_bf16_f32 v57, v42, v43\n"
    "v_cvt_pk_bf16_f32 v58, v44, v45\n"
    "v_cvt_pk_bf16_f32 v59, v46, v47\n"
    "s_nop 1\n"
    "v_mfma_f32_32x32x16_bf16 v[212:227], v[152:155], v[56:59], v[212:227]\n"
    "s_nop 11\n"
    "ds_write2st64_b32 v228, v212, v213 offset0:72 offset1:73\n"
    "ds_write2st64_b32 v228, v214, v215 offset0:74 offset1:75\n"
    "ds_write2st64_b32 v228, v216, v217 offset0:76 offset1:77\n"
    "ds_write2st64_b32 v228, v218, v219 offset0:78 offset1:79\n"
    "ds_write_b32 v228, v220 offset:20480\n"
    "ds_read_b128 v[128:131], v253 offset:0\n"
    "ds_read_b128 v[132:135], v253 offset:64\n"
    "ds_read_b128 v[136:139], v253 offset:2048\n"
    "ds_read_b128 v[140:143], v253 offset:2112\n"
    "ds_read_b128 v[144:147], v253 offset:4096\n"
    "ds_read_b128 v[148:151], v253 offset:4160\n"
    "s_waitcnt lgkmcnt(0)\n"
    "s_barrier\n"
    "ds_read2st64_b32 v[220:221], v230 offset0:72 offset1:81\n"
    "ds_read2st64_b32 v[222:223], v230 offset0:90 offset1:99\n"
    "ds_read2st64_b32 v[224:225], v230 offset0:108 offset1:117\n"
    "ds_read2st64_b32 v[226:227], v230 offset0:126 offset1:135\n"
    "ds_read2st64_b32 v[212:213], v229 offset0:72 offset1:81\n"
    "ds_read2st64_b32 v[214:215], v229 offset0:90 offset1:99\n"
    "ds_read2st64_b32 v[216:217], v229 offset0:108 offset1:117\n"
    "ds_read2st64_b32 v[218:219], v229 offset0:126 offset1:135\n"
    "s_waitcnt lgkmcnt(4)\n"
    "v_add_f32 v220, v220, v221\n"
    "v_add_f32 v222, v222, v223\n"
    "v_add_f32 v224, v224, v225\n"
    "v_add_f32 v226, v226, v227\n"
    "v_add_f32 v220, v220, v222\n"
    "v_add_f32 v224, v224, v226\n"
    "s_waitcnt lgkmcnt(0)\n"
    "v_add_f32 v212, v212, v213\n"
    "v_add_f32 v220, v220, v224\n"
    "v_add_f32 v214, v214, v215\n"
    "v_rcp_f32 v221, v220\n"
    "v_add_f32 v216, v216, v217\n"
    "v_add_f32 v218, v218, v219\n"
    "v_add_f32 v212, v212, v214\n"
    "v_add_f32 v216, v216, v218\n"
    "v_min_u32 v238, v238, v220\n"
    "v_add_f32 v212, v212, v216\n"
    "v_add_f32 v220, |v212|, v220\n"
    "v_mul_f32 v212, v212, v221\n"
    "v_max_u32 v239, v239, v220\n"
    "v_cvt_pk_bf16_f32 v212, v212, v212\n"
    "ds_write_b16 v231, v212 offset:2048\n"
    "s_waitcnt lgkmcnt(0)\n"
    "s_barrier\n"
    "ds_read_b128 v[188:191], v232 offset:2048\n"
    "ds_read_b128 v[192:195], v232 offset:3072\n"
    "s_waitcnt vmcnt(13)\n"
    "s_waitcnt lgkmcnt(1)\n"
    "v_mfma_f32_16x16x32_bf16 v[196:199], v[108:111], v[188:191], v[196:199]\n"
    "s_waitcnt vmcnt(12)\n"
    "v_mfma_f32_16x16x32_bf16 v[200:203], v[76:79], v[188:191], v[200:203]\n"
    "s_waitcnt vmcnt(11)\n"
    "v_mfma_f32_16x16x32_bf16 v[204:207], v[112:115], v[188:191], v[204:207]\n"
    "s_waitcnt vmcnt(10)\n"
    "v_mfma_f32_16x16x32_bf16 v[208:211], v[116:119], v[188:191], v[208:211]\n"
    "s_waitcnt vmcnt(9)\n"
    "s_waitcnt lgkmcnt(0)\n"
    "v_mfma_f32_16x16x32_bf16 v[196:199], v[80:83], v[192:195], v[196:199]\n"
    "s_waitcnt vmcnt(8)\n"
    "v_mfma_f32_16x16x32_bf16 v[200:203], v[120:123], v[192:195], v[200:203]\n"
    "s_waitcnt vmcnt(7)\n"
    "v_mfma_f32_16x16x32_bf16 v[204:207], v[124:127], v[192:195], v[204:207]\n"
    "s_waitcnt vmcnt(6)\n"
    "v_mfma_f32_16x16x32_bf16 v[208:211], v[84:87], v[192:195], v[208:211]\n"
    "s_waitcnt vmcnt(4)\n"
    "s_nop 6\n"
    "v_cndmask_b32 v196, v196, v204, %[upmask]\n"
    "v_cndmask_b32 v197, v197, v205, %[upmask]\n"
    "v_cndmask_b32 v198, v198, v206, %[upmask]\n"
    "v_cndmask_b32 v199, v199, v207, %[upmask]\n"
    "v_cndmask_b32 v200, v200, v208, %[upmask]\n"
    "v_cndmask_b32 v201, v201, v209, %[upmask]\n"
    "v_cndmask_b32 v202, v202, v210, %[upmask]\n"
    "v_cndmask_b32 v203, v203, v211, %[upmask]\n"
    "s_mov_b32 m0, %[m0s]\n"
                : "={v[196:203]}"(xa), "={v238}"(lmin), "={v239}"(lmax), "+{v[92:107]}"(v0), "+{v[108:123]}"(v1), "+{v[124:139]}"(v2), "+{v[140:155]}"(v3),
                  "+{v[236:237]}"(wsrc), "+{v[244:245]}"(look), "+{v246}"(p_tr), "+{v247}"(p_t), "+{v248}"(p_f), "={v[88:91]}"(xr), "={v[156:171]}"(wdA),
                  [st] "=&s"(st), [m0s] "=&s"(m0s), [sa] "=&s"(sa_), [sb] "=&s"(sb_)
                : "{v228}"(p_pw), "{v229}"(p_pr), "{v230}"(p_pl), "{v231}"(p_ow), "{v232}"(p_or), "{v233}"(p_q), "{v234}"(p_l), "{v235}"(p_v), "{v252}"(p_x), "{v253}"(p_m),
                  "{v[250:251]}"(vtag), [sfwd] "s"(o_sfwd), [swd] "s"(o_swd), [tdst] "s"(o_tdst), [upmask] "s"(o_upmask), [rs] "s"(wsr), [sk0] "s"(o_sk0), [sk4] "s"(o_sk4), [sv0] "s"(o_sv0), [sv4] "s"(o_sv4), [swo] "s"(o_swo), [sq] "s"(o_sq), [rot] "s"(o_rot), [tag] "s"(tag), [xb] "s"(o_xb), [spf] "s"(o_spf), [wdst] "s"(o_wdst), [wstep] "s"(o_wstep), [vmask] "s"(o_vmask)
                :
                "v0", "v1", "v2", "v3", "v4", "v5", "v6", "v7", "v8", "v9", "v10", "v11", "v12", "v13", "v14", "v15", "v16", "v17", "v18", "v19", "v20", "v21", "v22", "v23", "v24", "v25", "v26", "v27", "v28", "v29", "v30", "v31", "v32", "v33", "v34", "v35", "v36", "v37", "v38", "v39", "v40", "v41", "v42", "v43", "v44", "v45", "v46", "v47", "v48", "v49", "v50", "v51", "v52", "v53", "v54", "v55", "v56", "v57", "v58", "v59", "v60", "v61", "v62", "v63", "v64", "v65", "v66", "v67", "v68", "v69", "v70", "v71", "v72", "v73", "v74", "v75", "v76", "v77", "v78", "v79", "v80", "v81", "v82", "v83", "v84", "v85", "v86", "v87", "v172", "v173", "v174", "v175", "v176", "v177", "v178", "v179", "v180", "v181", "v182", "v183", "v184", "v185", "v186", "v187", "v188", "v189", "v190", "v191", "v192", "v193", "v194", "v195", "v212", "v213", "v214", "v215", "v216", "v217", "v218", "v219", "v220", "v221", "v222", "v223", "v224", "v225", "v226", "v227", "v240", "v241", "v242", "v243", "v204", "v205", "v206", "v207", "v208", "v209", "v210", "v211", "vcc"
                , "scc", "memory");
        }
        lane = lane_id();
        bool bad = lmin < 0x12800000u   || lmax >= 0x7b800000u  ;
        const int n = lane & 7, g = lane >> 4, up = (lane >> 3) & 1;
        unsigned xw[4]; f32x2 xf[4]; f32x2 sq;
        unsigned h_off = WS_H2 + (((tA + n) >> 4) * 16 + wid * 2 + up) * 1024 + (g * 16 + ((tA + n) & 15)) * 16;
        wd_early = __all(look == tag);
        const u32x2 xr0 = {xr[0], xr[1]}, xr1 = {xr[2], xr[3]};
#define F4(v, i) (f32x4){__uint_as_float(v[i]), __uint_as_float(v[i + 1]), __uint_as_float(v[i + 2]), __uint_as_float(v[i + 3])}
        const f32x4 ga0 = F4(v2, 4), ga1 = F4(v2, 8), sb0 = F4(v2, 12), sb1 = F4(v3, 0), cb0 = F4(v3, 4), cb1 = F4(v3, 8);
        for (int attempt = 0;; ++attempt) {
            const f32x2 y0 = bf2(xr0[0]) + (f32x2){ga0[0], ga0[1]} * (f32x2){xa[0], xa[1]}, y1 = bf2(xr0[1]) + (f32x2){ga0[2], ga0[3]} * (f32x2){xa[2], xa[3]};
            const f32x2 y2 = bf2(xr1[0]) + (f32x2){ga1[0], ga1[1]} * (f32x2){xa[4], xa[5]}, y3 = bf2(xr1[1]) + (f32x2){ga1[2], ga1[3]} * (f32x2){xa[6], xa[7]};
            xw[0] = cvtpk(y0[0], y0[1]); xw[1] = cvtpk(y1[0], y1[1]); xw[2] = cvtpk(y2[0], y2[1]); xw[3] = cvtpk(y3[0], y3[1]);
#pragma unroll
            for (int i = 0; i < 4; ++i) xf[i] = bf2(xw[i]);
            const f32x2 s2 = (xf[0] + xf[1]) + (xf[2] + xf[3]), q2 = (xf[0] * xf[0] + xf[1] * xf[1]) + (xf[2] * xf[2] + xf[3] * xf[3]);
            const float s = dpp_add<0x128>(s2[0] + s2[1]), q = dpp_add<0x128>(q2[0] + q2[1]);
            const auto sw = __builtin_amdgcn_permlane16_swap(__float_as_uint(s), __float_as_uint(q), false, false);
            const float t = swap32_sum(__uint_as_float(sw[0]) + __uint_as_float(sw[1]));
            if (!(lane & 40)) *(float*)(smem + L_ST + (n * 8 + wid) * 8 + (g & 1) * 4) = t;
            if (bad) *(int*)(smem + L_FLAG) = 1;
            {
                const int tok = tA + n;
                const auto x0 = __builtin_amdgcn_permlane16_swap(xw[0], xw[2], false, false), x1 = __builtin_amdgcn_permlane16_swap(xw[1], xw[3], false, false);
                st16_sc1(wsr, WS_X2 + (tok * DM + wid * 64 + up * 32 + (g & 1) * 16 + (g >> 1) * 8) * 2, (u32x4){x0[0], x1[0], x0[1], x1[1]});
            }
            PIN(h_off);
            LDS_BAR();
            const int flag = *(const volatile LAS int*)(lds + L_FLAG);
            f32x4 sv[4];
#pragma unroll
            for (int i = 0; i < 4; ++i) sv[i] = *(const f32x4*)(smem + L_ST + n * 64 + i * 16);
            const f32x4 t4 = (sv[0] + sv[1]) + (sv[2] + sv[3]);
            sq = (f32x2){t4[0], t4[1]} + (f32x2){t4[2], t4[3]};
            if (__builtin_expect(__builtin_amdgcn_readfirstlane(flag) == 0 || attempt, 1)) break;
            int lane = lane_id();
            bad = false;
            f32x4 xs[4];
#pragma unroll
            for (int i = 0; i < 4; ++i) xs[i] = (f32x4){0.f, 0.f, 0.f, 0.f};
            const int q32 = lane & 31, hf = lane >> 5;
            const unsigned one = (q32 & 27) == 16 ? 0x3f803f80u : 0u;
            for (int j = 0; j < 8; ++j) {
                const u32x4 qf = ld16_sc1(wsr, WS_Q + (tA >> 3) * 8192 + j * 1024 + lane * 16);
                const f32x16 z = {};
                float m = -INFINITY;
                for (int t = 0; t < 8; ++t) {
                    const f32x16 sc = mfma32(ld16_sc1(wsr, WS_KT + (wid * 8 + t) * 1024 + lane * 16), qf, z);
#pragma unroll
                    for (int r = 0; r < 16; ++r) m = fmaxf(m, sc[r]);
                }
                m = swap32_max(m);
                if (hf == 0) *(float*)(smem + L_MX + (wid * 32 + q32) * 4) = m;
                LDS_BAR();
#pragma unroll
                for (int w = 0; w < 8; ++w) m = fmaxf(m, *(const float*)(smem + L_MX + (w * 32 + q32) * 4));
                f32x16 o = {};
                for (int t = 0; t < 8; ++t) {
                    const f32x16 sc = mfma32(ld16_sc1(wsr, WS_KT + (wid * 8 + t) * 1024 + lane * 16), qf, z);
#pragma unroll
                    for (int s_ = 0; s_ < 2; ++s_) {
                        u32x4 vf = ld16_sc1(wsr, WS_VT + (wid * 8 + t) * 1024 + s_ * 512 + (hf * 16 + (q32 & 15)) * 16);
                        if (q32 >= 16) vf = (u32x4){one, one, one, one};
                        u32x4 p;
#pragma unroll
                        for (int e = 0; e < 4; ++e) p[e] = cvtpk(__builtin_amdgcn_exp2f(sc[8 * s_ + 2 * e] - m), __builtin_amdgcn_exp2f(sc[8 * s_ + 2 * e + 1] - m));
                        o = mfma32(vf, p, o);
                    }
                }
#pragma unroll
                for (int r = 0; r < 9; ++r) *(float*)(smem + L_PART + ((wid * 9 + r) * 64 + lane) * 4) = o[r];
                LDS_BAR();
                float so = 0.f, sl = 0.f;
#pragma unroll
                for (int w = 0; w < 8; ++w) { so += *(const float*)(smem + L_PART + ((w * 9 + wid) * 64 + lane) * 4); sl += *(const float*)(smem + L_PART + ((w * 9 + 8) * 64 + lane) * 4); }
                const float r = so * __builtin_amdgcn_rcpf(sl);
                *(uint16_t*)(smem + L_OB + (q32 >> 4) * 1024 + ((((q32 >> 3) & 1) * 2 + (wid >> 2)) * 16 + (q32 & 7)) * 16 + ((wid & 3) + 4 * hf) * 2) = (uint16_t)cvtpk(r, r);
                LDS_BAR();
#pragma unroll
                for (int i = 0; i < 8; ++i)
                    xs[i >> 1] = mfma16(ld16_sc1(wsr, WS_WO + j * 65536 + wid * 8192 + i * 1024 + lane * 16), *(const u32x4*)(smem + L_OB + (i & 1) * 1024 + ((lane & 48) + (lane & 7)) * 16), xs[i >> 1]);
            }
#pragma unroll
            for (int i = 0; i < 8; ++i) { float lo = xs[i >> 2][i & 3], hi = xs[2 + (i >> 2)][i & 3]; PIN(lo); PIN(hi); xa[i] = (lane & 8) ? hi : lo; }
        }
        const float mean = sq[0] * (1.f / 512.f);
        const float rstd = __builtin_amdgcn_rsqf(fmaxf(sq[1] * (1.f / 512.f) - mean * mean, 0.f) + 1e-5f);
        const f32x2 h0 = (xf[0] - mean) * ((f32x2){cb0[0], cb0[1]} * rstd) + (f32x2){sb0[0], sb0[1]}, h1 = (xf[1] - mean) * ((f32x2){cb0[2], cb0[3]} * rstd) + (f32x2){sb0[2], sb0[3]};
        const f32x2 h2 = (xf[2] - mean) * ((f32x2){cb1[0], cb1[1]} * rstd) + (f32x2){sb1[0], sb1[1]}, h3 = (xf[3] - mean) * ((f32x2){cb1[2], cb1[3]} * rstd) + (f32x2){sb1[2], sb1[3]};
        const unsigned hw[4] = {cvtpk(h0[0], h0[1]), cvtpk(h1[0], h1[1]), cvtpk(h2[0], h2[1]), cvtpk(h3[0], h3[1])};
        {
            const auto u0 = __builtin_amdgcn_permlane32_swap(hw[0], hw[2], false, false), u1 = __builtin_amdgcn_permlane32_swap(hw[1], hw[3], false, false);
            st16_sc1(wsr, h_off, (u32x4){u0[0], u1[0], u0[1], u1[1]});
        }
        if (__builtin_expect(!wd_early, 0)) poll8(wsr, WS_FWD + ((nt * 112 + wid * 14) * 2 + (lane < 28 ? lane : 27)) * 8, tag);
        VM_WAIT(0);
        __builtin_amdgcn_s_barrier();
        if (wid == 0 && lane < 32) st8_sc1(wsr, WS_F3 + lane * 8192 + cw * 32, tag);
        if (b == 0 && wid == 1 && lane == 0) st8_sc1(wsr, WS_EPOCH, tag);
    }
    u32x4 wdf[14][2], gbv[4];
    {
        int lane = lane_id();
        const int g = lane >> 4, q = (lane & 15) >> 2, pp = lane & 3;
        unsigned wl = (16 * (g >> 1) + 4 * (g & 1) + q) * 224 + pp * 8, wm = wl + (pp < 2 ? 96 : 192);
        unsigned l16b = lane * 16;
        asm volatile("" : "+{v244}"(l16b), "+{v245}"(wl), "+{v246}"(wm));
        unsigned hs = WS_H2 + (xg * 16 + wid * 2) * 16384, ds = WS_WD + (nt * 112 + wid * 14) * 2048;
        asm volatile("" : "+s"(hs), "+s"(ds));
        if (__builtin_expect(!wd_early, 0)) {
#define WD4(v, f) do { const u32x4 a = ld16_sc1(wsr, ds + (f) * 1024 + l16b), b_ = ld16_sc1(wsr, ds + (f) * 1024 + 1024 + l16b), c = ld16_sc1(wsr, ds + (f) * 1024 + 2048 + l16b), d = ld16_sc1(wsr, ds + (f) * 1024 + 3072 + l16b); \
                v = (u32x16){a[0], a[1], a[2], a[3], b_[0], b_[1], b_[2], b_[3], c[0], c[1], c[2], c[3], d[0], d[1], d[2], d[3]}; } while (0)
            WD4(wdA, 0);
        }
        asm volatile("buffer_load_dwordx4 v[188:191], %2, %3, %4 offen sc1\n\tbuffer_load_dwordx4 v[192:195], %2, %3, %4 offen offset:1024 sc1\n\t"
                     "buffer_load_dwordx4 v[196:199], %2, %3, %4 offen offset:2048 sc1\n\tbuffer_load_dwordx4 v[200:203], %2, %3, %4 offen offset:3072 sc1\n\t"
                     "buffer_load_dwordx4 v[204:207], %2, %3, %5 offen sc1\n\tbuffer_load_dwordx4 v[208:211], %2, %3, %5 offen offset:1024 sc1\n\t"
                     "buffer_load_dwordx4 v[212:215], %2, %3, %5 offen offset:2048 sc1\n\tbuffer_load_dwordx4 v[216:219], %2, %3, %5 offen offset:3072 sc1"
                     : "={v[188:203]}"(wdE), "={v[204:219]}"(wdF) : "v"(l16b), "s"(wsr), "s"(WS_WD + (nt * 112 + wid * 14) * 2048 + 16384), "s"(WS_WD + (nt * 112 + wid * 14) * 2048 + 20480) : "memory");
        poll8(wsr, WS_F3 + idx * 8192 + (xg * 32 + 4 * wid + (lane & 3)) * 32, tag);
        f32x16 ac0, ac1, ac2; f32x8 ac3;
        {
            unsigned st;
            asm volatile(
    "s_add_u32 %[st], %[hs], 0\n"
    "buffer_load_dwordx4 v[116:119], v244, %[rs], %[st] offen sc1\n"
    "s_add_u32 %[st], %[hs], 16384\n"
    "buffer_load_dwordx4 v[120:123], v244, %[rs], %[st] offen sc1\n"
    "s_add_u32 %[st], %[hs], 1024\n"
    "buffer_load_dwordx4 v[124:127], v244, %[rs], %[st] offen sc1\n"
    "s_add_u32 %[st], %[hs], 17408\n"
    "buffer_load_dwordx4 v[128:131], v244, %[rs], %[st] offen sc1\n"
    "s_add_u32 %[st], %[hs], 2048\n"
    "buffer_load_dwordx4 v[132:135], v244, %[rs], %[st] offen sc1\n"
    "s_add_u32 %[st], %[hs], 18432\n"
    "buffer_load_dwordx4 v[136:139], v244, %[rs], %[st] offen sc1\n"
    "s_add_u32 %[st], %[hs], 3072\n"
    "buffer_load_dwordx4 v[140:143], v244, %[rs], %[st] offen sc1\n"
    "s_add_u32 %[st], %[hs], 19456\n"
    "buffer_load_dwordx4 v[144:147], v244, %[rs], %[st] offen sc1\n"
    "s_add_u32 %[st], %[hs], 4096\n"
    "buffer_load_dwordx4 v[148:151], v244, %[rs], %[st] offen sc1\n"
    "s_add_u32 %[st], %[hs], 20480\n"
    "buffer_load_dwordx4 v[152:155], v244, %[rs], %[st] offen sc1\n"
    "v_add_u32 v247, 57344, v245\n"
    "v_add_u32 v248, 57344, v246\n"
    "ds_read_b64_tr_b16 v[88:89], v245 offset:0\n"
    "ds_read_b64_tr_b16 v[90:91], v245 offset:1792\n"
    "ds_read_b64_tr_b16 v[92:93], v245 offset:112\n"
    "ds_read_b64_tr_b16 v[94:95], v245 offset:1904\n"
    "ds_read_b64_tr_b16 v[96:97], v245 offset:32\n"
    "ds_read_b64_tr_b16 v[98:99], v245 offset:1824\n"
    "ds_read_b64_tr_b16 v[100:101], v245 offset:144\n"
    "ds_read_b64_tr_b16 v[102:103], v245 offset:1936\n"
    "ds_read_b64_tr_b16 v[104:105], v245 offset:64\n"
    "ds_read_b64_tr_b16 v[106:107], v245 offset:1856\n"
    "ds_read_b64_tr_b16 v[108:109], v245 offset:176\n"
    "ds_read_b64_tr_b16 v[110:111], v245 offset:1968\n"
    "ds_read_b64_tr_b16 v[112:113], v246 offset:0\n"
    "ds_read_b64_tr_b16 v[114:115], v246 offset:1792\n"
    "s_waitcnt vmcnt(8)\n"
    "s_waitcnt lgkmcnt(12)\n"
    "v_mfma_f32_16x16x32_bf16 v[32:35], v[88:91], v[116:119], 0\n"
    "v_mfma_f32_16x16x32_bf16 v[36:39], v[88:91], v[120:123], 0\n"
    "ds_read_b64_tr_b16 v[88:89], v245 offset:7168\n"
    "s_waitcnt lgkmcnt(11)\n"
    "v_mfma_f32_16x16x32_bf16 v[40:43], v[92:95], v[116:119], 0\n"
    "ds_read_b64_tr_b16 v[90:91], v245 offset:8960\n"
    "v_mfma_f32_16x16x32_bf16 v[44:47], v[92:95], v[120:123], 0\n"
    "ds_read_b64_tr_b16 v[92:93], v245 offset:7280\n"
    "s_add_u32 %[st], %[hs], 5120\n"
    "buffer_load_dwordx4 v[236:239], v244, %[rs], %[st] offen sc1\n"
    "s_add_u32 %[st], %[hs], 21504\n"
    "buffer_load_dwordx4 v[240:243], v244, %[rs], %[st] offen sc1\n"
    "s_waitcnt lgkmcnt(11)\n"
    "v_mfma_f32_16x16x32_bf16 v[48:51], v[96:99], v[116:119], 0\n"
    "ds_read_b64_tr_b16 v[94:95], v245 offset:9072\n"
    "v_mfma_f32_16x16x32_bf16 v[52:55], v[96:99], v[120:123], 0\n"
    "ds_read_b64_tr_b16 v[96:97], v245 offset:7200\n"
    "s_waitcnt lgkmcnt(11)\n"
    "v_mfma_f32_16x16x32_bf16 v[56:59], v[100:103], v[116:119], 0\n"
    "ds_read_b64_tr_b16 v[98:99], v245 offset:8992\n"
    "v_mfma_f32_16x16x32_bf16 v[60:63], v[100:103], v[120:123], 0\n"
    "ds_read_b64_tr_b16 v[100:101], v245 offset:7312\n"
    "s_waitcnt lgkmcnt(11)\n"
    "v_mfma_f32_16x16x32_bf16 v[64:67], v[104:107], v[116:119], 0\n"
    "ds_read_b64_tr_b16 v[102:103], v245 offset:9104\n"
    "v_mfma_f32_16x16x32_bf16 v[68:71], v[104:107], v[120:123], 0\n"
    "ds_read_b64_tr_b16 v[104:105], v245 offset:7232\n"
    "s_waitcnt lgkmcnt(11)\n"
    "v_mfma_f32_16x16x32_bf16 v[72:75], v[108:111], v[116:119], 0\n"
    "ds_read_b64_tr_b16 v[106:107], v245 offset:9024\n"
    "v_mfma_f32_16x16x32_bf16 v[76:79], v[108:111], v[120:123], 0\n"
    "ds_read_b64_tr_b16 v[108:109], v245 offset:7344\n"
    "s_waitcnt lgkmcnt(11)\n"
    "v_mfma_f32_16x16x32_bf16 v[80:83], v[112:115], v[116:119], 0\n"
    "ds_read_b64_tr_b16 v[110:111], v245 offset:9136\n"
    "v_mfma_f32_16x16x32_bf16 v[84:87], v[112:115], v[120:123], 0\n"
    "ds_read_b64_tr_b16 v[112:113], v246 offset:7168\n"
    "s_waitcnt vmcnt(8)\n"
    "s_waitcnt lgkmcnt(7)\n"
    "v_mfma_f32_16x16x32_bf16 v[32:35], v[88:91], v[124:127], v[32:35]\n"
    "ds_read_b64_tr_b16 v[114:115], v246 offset:8960\n"
    "v_mfma_f32_16x16x32_bf16 v[36:39], v[88:91], v[128:131], v[36:39]\n"
    "ds_read_b64_tr_b16 v[88:89], v245 offset:14336\n"
    "v_mfma_f32_16x16x32_bf16 v[40:43], v[92:95], v[124:127], v[40:43]\n"
    "ds_read_b64_tr_b16 v[90:91], v245 offset:16128\n"
    "v_mfma_f32_16x16x32_bf16 v[44:47], v[92:95], v[128:131], v[44:47]\n"
    "ds_read_b64_tr_b16 v[92:93], v245 offset:14448\n"
    "s_add_u32 %[st], %[hs], 6144\n"
    "buffer_load_dwordx4 v[116:119], v244, %[rs], %[st] offen sc1\n"
    "s_add_u32 %[st], %[hs], 22528\n"
    "buffer_load_dwordx4 v[120:123], v244, %[rs], %[st] offen sc1\n"
    "v_mfma_f32_16x16x32_bf16 v[48:51], v[96:99], v[124:127], v[48:51]\n"
    "ds_read_b64_tr_b16 v[94:95], v245 offset:16240\n"
    "v_mfma_f32_16x16x32_bf16 v[52:55], v[96:99], v[128:131], v[52:55]\n"
    "ds_read_b64_tr_b16 v[96:97], v245 offset:14368\n"
    "s_waitcnt lgkmcnt(9)\n"
    "v_mfma_f32_16x16x32_bf16 v[56:59], v[100:103], v[124:127], v[56:59]\n"
    "ds_read_b64_tr_b16 v[98:99], v245 offset:16160\n"
    "v_mfma_f32_16x16x32_bf16 v[60:63], v[100:103], v[128:131], v[60:63]\n"
    "ds_read_b64_tr_b16 v[100:101], v245 offset:14480\n"
    "v_mfma_f32_16x16x32_bf16 v[64:67], v[104:107], v[124:127], v[64:67]\n"
    "ds_read_b64_tr_b16 v[102:103], v245 offset:16272\n"
    "v_mfma_f32_16x16x32_bf16 v[68:71], v[104:107], v[128:131], v[68:71]\n"
    "ds_read_b64_tr_b16 v[104:105], v245 offset:14400\n"
    "s_waitcnt lgkmcnt(9)\n"
    "v_mfma_f32_16x16x32_bf16 v[72:75], v[108:111], v[124:127], v[72:75]\n"
    "ds_read_b64_tr_b16 v[106:107], v245 offset:16192\n"
    "v_mfma_f32_16x16x32_bf16 v[76:79], v[108:111], v[128:131], v[76:79]\n"
    "ds_read_b64_tr_b16 v[108:109], v245 offset:14512\n"
    "v_mfma_f32_16x16x32_bf16 v[80:83], v[112:115], v[124:127], v[80:83]\n"
    "ds_read_b64_tr_b16 v[110:111], v245 offset:16304\n"
    "v_mfma_f32_16x16x32_bf16 v[84:87], v[112:115], v[128:131], v[84:87]\n"
    "ds_read_b64_tr_b16 v[112:113], v246 offset:14336\n"
    "s_waitcnt vmcnt(8)\n"
    "s_waitcnt lgkmcnt(7)\n"
    "v_mfma_f32_16x16x32_bf16 v[32:35], v[88:91], v[132:135], v[32:35]\n"
    "ds_read_b64_tr_b16 v[114:115], v246 offset:16128\n"
    "v_mfma_f32_16x16x32_bf16 v[36:39], v[88:91], v[136:139], v[36:39]\n"
    "ds_read_b64_tr_b16 v[88:89], v245 offset:21504\n"
    "v_mfma_f32_16x16x32_bf16 v[40:43], v[92:95], v[132:135], v[40:43]\n"
    "ds_read_b64_tr_b16 v[90:91], v245 offset:23296\n"
    "v_mfma_f32_16x16x32_bf16 v[44:47], v[92:95], v[136:139], v[44:47]\n"
    "ds_read_b64_tr_b16 v[92:93], v245 offset:21616\n"
    "s_add_u32 %[st], %[hs], 7168\n"
    "buffer_load_dwordx4 v[124:127], v244, %[rs], %[st] offen sc1\n"
    "s_add_u32 %[st], %[hs], 23552\n"
    "buffer_load_dwordx4 v[128:131], v244, %[rs], %[st] offen sc1\n"
    "v_mfma_f32_16x16x32_bf16 v[48:51], v[96:99], v[132:135], v[48:51]\n"
    "ds_read_b64_tr_b16 v[94:95], v245 offset:23408\n"
    "v_mfma_f32_16x16x32_bf16 v[52:55], v[96:99], v[136:139], v[52:55]\n"
    "ds_read_b64_tr_b16 v[96:97], v245 offset:21536\n"
    "s_waitcnt lgkmcnt(9)\n"
    "v_mfma_f32_16x16x32_bf16 v[56:59], v[100:103], v[132:135], v[56:59]\n"
    "ds_read_b64_tr_b16 v[98:99], v245 offset:23328\n"
    "v_mfma_f32_16x16x32_bf16 v[60:63], v[100:103], v[136:139], v[60:63]\n"
    "ds_read_b64_tr_b16 v[100:101], v245 offset:21648\n"
    "v_mfma_f32_16x16x32_bf16 v[64:67], v[104:107], v[132:135], v[64:67]\n"
    "ds_read_b64_tr_b16 v[102:103], v245 offset:23440\n"
    "v_mfma_f32_16x16x32_bf16 v[68:71], v[104:107], v[136:139], v[68:71]\n"
    "ds_read_b64_tr_b16 v[104:105], v245 offset:21568\n"
    "s_waitcnt lgkmcnt(9)\n"
    "v_mfma_f32_16x16x32_bf16 v[72:75], v[108:111], v[132:135], v[72:75]\n"
    "ds_read_b64_tr_b16 v[106:107], v245 offset:23360\n"
    "v_mfma_f32_16x16x32_bf16 v[76:79], v[108:111], v[136:139], v[76:79]\n"
    "ds_read_b64_tr_b16 v[108:109], v245 offset:21680\n"
    "v_mfma_f32_16x16x32_bf16 v[80:83], v[112:115], v[132:135], v[80:83]\n"
    "ds_read_b64_tr_b16 v[110:111], v245 offset:23472\n"
    "v_mfma_f32_16x16x32_bf16 v[84:87], v[112:115], v[136:139], v[84:87]\n"
    "ds_read_b64_tr_b16 v[112:113], v246 offset:21504\n"
    "s_waitcnt vmcnt(8)\n"
    "s_waitcnt lgkmcnt(7)\n"
    "v_mfma_f32_16x16x32_bf16 v[32:35], v[88:91], v[140:143], v[32:35]\n"
    "ds_read_b64_tr_b16 v[114:115], v246 offset:23296\n"
    "v_mfma_f32_16x16x32_bf16 v[36:39], v[88:91], v[144:147], v[36:39]\n"
    "ds_read_b64_tr_b16 v[88:89], v245 offset:28672\n"
    "v_mfma_f32_16x16x32_bf16 v[40:43], v[92:95], v[140:143], v[40:43]\n"
    "ds_read_b64_tr_b16 v[90:91], v245 offset:30464\n"
    "v_mfma_f32_16x16x32_bf16 v[44:47], v[92:95], v[144:147], v[44:47]\n"
    "ds_read_b64_tr_b16 v[92:93], v245 offset:28784\n"
    "s_add_u32 %[st], %[hs], 8192\n"
    "buffer_load_dwordx4 v[132:135], v244, %[rs], %[st] offen sc1\n"
    "s_add_u32 %[st], %[hs], 24576\n"
    "buffer_load_dwordx4 v[136:139], v244, %[rs], %[st] offen sc1\n"
    "v_mfma_f32_16x16x32_bf16 v[48:51], v[96:99], v[140:143], v[48:51]\n"
    "ds_read_b64_tr_b16 v[94:95], v245 offset:30576\n"
    "v_mfma_f32_16x16x32_bf16 v[52:55], v[96:99], v[144:147], v[52:55]\n"
    "ds_read_b64_tr_b16 v[96:97], v245 offset:28704\n"
    "s_waitcnt lgkmcnt(9)\n"
    "v_mfma_f32_16x16x32_bf16 v[56:59], v[100:103], v[140:143], v[56:59]\n"
    "ds_read_b64_tr_b16 v[98:99], v245 offset:30496\n"
    "v_mfma_f32_16x16x32_bf16 v[60:63], v[100:103], v[144:147], v[60:63]\n"
    "ds_read_b64_tr_b16 v[100:101], v245 offset:28816\n"
    "v_mfma_f32_16x16x32_bf16 v[64:67], v[104:107], v[140:143], v[64:67]\n"
    "ds_read_b64_tr_b16 v[102:103], v245 offset:30608\n"
    "v_mfma_f32_16x16x32_bf16 v[68:71], v[104:107], v[144:147], v[68:71]\n"
    "ds_read_b64_tr_b16 v[104:105], v245 offset:28736\n"
    "s_waitcnt lgkmcnt(9)\n"
    "v_mfma_f32_16x16x32_bf16 v[72:75], v[108:111], v[140:143], v[72:75]\n"
    "ds_read_b64_tr_b16 v[106:107], v245 offset:30528\n"
    "v_mfma_f32_16x16x32_bf16 v[76:79], v[108:111], v[144:147], v[76:79]\n"
    "ds_read_b64_tr_b16 v[108:109], v245 offset:28848\n"
    "v_mfma_f32_16x16x32_bf16 v[80:83], v[112:115], v[140:143], v[80:83]\n"
    "ds_read_b64_tr_b16 v[110:111], v245 offset:30640\n"
    "v_mfma_f32_16x16x32_bf16 v[84:87], v[112:115], v[144:147], v[84:87]\n"
    "ds_read_b64_tr_b16 v[112:113], v246 offset:28672\n"
    "s_waitcnt vmcnt(8)\n"
    "s_waitcnt lgkmcnt(7)\n"
    "v_mfma_f32_16x16x32_bf16 v[32:35], v[88:91], v[148:151], v[32:35]\n"
    "ds_read_b64_tr_b16 v[114:115], v246 offset:30464\n"
    "v_mfma_f32_16x16x32_bf16 v[36:39], v[88:91], v[152:155], v[36:39]\n"
    "ds_read_b64_tr_b16 v[88:89], v245 offset:35840\n"
    "v_mfma_f32_16x16x32_bf16 v[40:43], v[92:95], v[148:151], v[40:43]\n"
    "ds_read_b64_tr_b16 v[90:91], v245 offset:37632\n"
    "v_mfma_f32_16x16x32_bf16 v[44:47], v[92:95], v[152:155], v[44:47]\n"
    "ds_read_b64_tr_b16 v[92:93], v245 offset:35952\n"
    "s_add_u32 %[st], %[hs], 9216\n"
    "buffer_load_dwordx4 v[140:143], v244, %[rs], %[st] offen sc1\n"
    "s_add_u32 %[st], %[hs], 25600\n"
    "buffer_load_dwordx4 v[144:147], v244, %[rs], %[st] offen sc1\n"
    "v_mfma_f32_16x16x32_bf16 v[48:51], v[96:99], v[148:151], v[48:51]\n"
    "ds_read_b64_tr_b16 v[94:95], v245 offset:37744\n"
    "v_mfma_f32_16x16x32_bf16 v[52:55], v[96:99], v[152:155], v[52:55]\n"
    "ds_read_b64_tr_b16 v[96:97], v245 offset:35872\n"
    "s_waitcnt lgkmcnt(9)\n"
    "v_mfma_f32_16x16x32_bf16 v[56:59], v[100:103], v[148:151], v[56:59]\n"
    "ds_read_b64_tr_b16 v[98:99], v245 offset:37664\n"
    "v_mfma_f32_16x16x32_bf16 v[60:63], v[100:103], v[152:155], v[60:63]\n"
    "ds_read_b64_tr_b16 v[100:101], v245 offset:35984\n"
    "v_mfma_f32_16x16x32_bf16 v[64:67], v[104:107], v[148:151], v[64:67]\n"
    "ds_read_b64_tr_b16 v[102:103], v245 offset:37776\n"
    "v_mfma_f32_16x16x32_bf16 v[68:71], v[104:107], v[152:155], v[68:71]\n"
    "ds_read_b64_tr_b16 v[104:105], v245 offset:35904\n"
    "s_waitcnt lgkmcnt(9)\n"
    "v_mfma_f32_16x16x32_bf16 v[72:75], v[108:111], v[148:151], v[72:75]\n"
    "ds_read_b64_tr_b16 v[106:107], v245 offset:37696\n"
    "v_mfma_f32_16x16x32_bf16 v[76:79], v[108:111], v[152:155], v[76:79]\n"
    "ds_read_b64_tr_b16 v[108:109], v245 offset:36016\n"
    "v_mfma_f32_16x16x32_bf16 v[80:83], v[112:115], v[148:151], v[80:83]\n"
    "ds_read_b64_tr_b16 v[110:111], v245 offset:37808\n"
    "v_mfma_f32_16x16x32_bf16 v[84:87], v[112:115], v[152:155], v[84:87]\n"
    "ds_read_b64_tr_b16 v[112:113], v246 offset:35840\n"
    "s_waitcnt vmcnt(8)\n"
    "s_waitcnt lgkmcnt(7)\n"
    "v_mfma_f32_16x16x32_bf16 v[32:35], v[88:91], v[236:239], v[32:35]\n"
    "ds_read_b64_tr_b16 v[114:115], v246 offset:37632\n"
    "v_mfma_f32_16x16x32_bf16 v[36:39], v[88:91], v[240:243], v[36:39]\n"
    "ds_read_b64_tr_b16 v[88:89], v245 offset:43008\n"
    "v_mfma_f32_16x16x32_bf16 v[40:43], v[92:95], v[236:239], v[40:43]\n"
    "ds_read_b64_tr_b16 v[90:91], v245 offset:44800\n"
    "v_mfma_f32_16x16x32_bf16 v[44:47], v[92:95], v[240:243], v[44:47]\n"
    "ds_read_b64_tr_b16 v[92:93], v245 offset:43120\n"
    "s_add_u32 %[st], %[hs], 10240\n"
    "buffer_load_dwordx4 v[148:151], v244, %[rs], %[st] offen sc1\n"
    "s_add_u32 %[st], %[hs], 26624\n"
    "buffer_load_dwordx4 v[152:155], v244, %[rs], %[st] offen sc1\n"
    "v_mfma_f32_16x16x32_bf16 v[48:51], v[96:99], v[236:239], v[48:51]\n"
    "ds_read_b64_tr_b16 v[94:95], v245 offset:44912\n"
    "v_mfma_f32_16x16x32_bf16 v[52:55], v[96:99], v[240:243], v[52:55]\n"
    "ds_read_b64_tr_b16 v[96:97], v245 offset:43040\n"
    "s_waitcnt lgkmcnt(9)\n"
    "v_mfma_f32_16x16x32_bf16 v[56:59], v[100:103], v[236:239], v[56:59]\n"
    "ds_read_b64_tr_b16 v[98:99], v245 offset:44832\n"
    "v_mfma_f32_16x16x32_bf16 v[60:63], v[100:103], v[240:243], v[60:63]\n"
    "ds_read_b64_tr_b16 v[100:101], v245 offset:43152\n"
    "v_mfma_f32_16x16x32_bf16 v[64:67], v[104:107], v[236:239], v[64:67]\n"
    "ds_read_b64_tr_b16 v[102:103], v245 offset:44944\n"
    "v_mfma_f32_16x16x32_bf16 v[68:71], v[104:107], v[240:243], v[68:71]\n"
    "ds_read_b64_tr_b16 v[104:105], v245 offset:43072\n"
    "s_waitcnt lgkmcnt(9)\n"
    "v_mfma_f32_16x16x32_bf16 v[72:75], v[108:111], v[236:239], v[72:75]\n"
    "ds_read_b64_tr_b16 v[106:107], v245 offset:44864\n"
    "v_mfma_f32_16x16x32_bf16 v[76:79], v[108:111], v[240:243], v[76:79]\n"
    "ds_read_b64_tr_b16 v[108:109], v245 offset:43184\n"
    "v_mfma_f32_16x16x32_bf16 v[80:83], v[112:115], v[236:239], v[80:83]\n"
    "ds_read_b64_tr_b16 v[110:111], v245 offset:44976\n"
    "v_mfma_f32_16x16x32_bf16 v[84:87], v[112:115], v[240:243], v[84:87]\n"
    "ds_read_b64_tr_b16 v[112:113], v246 offset:43008\n"
    "s_waitcnt vmcnt(8)\n"
    "s_waitcnt lgkmcnt(7)\n"
    "v_mfma_f32_16x16x32_bf16 v[32:35], v[88:91], v[116:119], v[32:35]\n"
    "ds_read_b64_tr_b16 v[114:115], v246 offset:44800\n"
    "v_mfma_f32_16x16x32_bf16 v[36:39], v[88:91], v[120:123], v[36:39]\n"
    "ds_read_b64_tr_b16 v[88:89], v245 offset:50176\n"
    "v_mfma_f32_16x16x32_bf16 v[40:43], v[92:95], v[116:119], v[40:43]\n"
    "ds_read_b64_tr_b16 v[90:91], v245 offset:51968\n"
    "v_mfma_f32_16x16x32_bf16 v[44:47], v[92:95], v[120:123], v[44:47]\n"
    "ds_read_b64_tr_b16 v[92:93], v245 offset:50288\n"
    "s_add_u32 %[st], %[hs], 11264\n"
    "buffer_load_dwordx4 v[236:239], v244, %[rs], %[st] offen sc1\n"
    "s_add_u32 %[st], %[hs], 27648\n"
    "buffer_load_dwordx4 v[240:243], v244, %[rs], %[st] offen sc1\n"
    "v_mfma_f32_16x16x32_bf16 v[48:51], v[96:99], v[116:119], v[48:51]\n"
    "ds_read_b64_tr_b16 v[94:95], v245 offset:52080\n"
    "v_mfma_f32_16x16x32_bf16 v[52:55], v[96:99], v[120:123], v[52:55]\n"
    "ds_read_b64_tr_b16 v[96:97], v245 offset:50208\n"
    "s_waitcnt lgkmcnt(9)\n"
    "v_mfma_f32_16x16x32_bf16 v[56:59], v[100:103], v[116:119], v[56:59]\n"
    "ds_read_b64_tr_b16 v[98:99], v245 offset:52000\n"
    "v_mfma_f32_16x16x32_bf16 v[60:63], v[100:103], v[120:123], v[60:63]\n"
    "ds_read_b64_tr_b16 v[100:101], v245 offset:50320\n"
    "v_mfma_f32_16x16x32_bf16 v[64:67], v[104:107], v[116:119], v[64:67]\n"
    "ds_read_b64_tr_b16 v[102:103], v245 offset:52112\n"
    "v_mfma_f32_16x16x32_bf16 v[68:71], v[104:107], v[120:123], v[68:71]\n"
    "ds_read_b64_tr_b16 v[104:105], v245 offset:50240\n"
    "s_waitcnt lgkmcnt(9)\n"
    "v_mfma_f32_16x16x32_bf16 v[72:75], v[108:111], v[116:119], v[72:75]\n"
    "ds_read_b64_tr_b16 v[106:107], v245 offset:52032\n"
    "v_mfma_f32_16x16x32_bf16 v[76:79], v[108:111], v[120:123], v[76:79]\n"
    "ds_read_b64_tr_b16 v[108:109], v245 offset:50352\n"
    "v_mfma_f32_16x16x32_bf16 v[80:83], v[112:115], v[116:119], v[80:83]\n"
    "ds_read_b64_tr_b16 v[110:111], v245 offset:52144\n"
    "v_mfma_f32_16x16x32_bf16 v[84:87], v[112:115], v[120:123], v[84:87]\n"
    "ds_read_b64_tr_b16 v[112:113], v246 offset:50176\n"
    "s_waitcnt vmcnt(8)\n"
    "s_waitcnt lgkmcnt(7)\n"
    "v_mfma_f32_16x16x32_bf16 v[32:35], v[88:91], v[124:127], v[32:35]\n"
    "ds_read_b64_tr_b16 v[114:115], v246 offset:51968\n"
    "v_mfma_f32_16x16x32_bf16 v[36:39], v[88:91], v[128:131], v[36:39]\n"
    "ds_read_b64_tr_b16 v[88:89], v247 offset:0\n"
    "v_mfma_f32_16x16x32_bf16 v[40:43], v[92:95], v[124:127], v[40:43]\n"
    "ds_read_b64_tr_b16 v[90:91], v247 offset:1792\n"
    "v_mfma_f32_16x16x32_bf16 v[44:47], v[92:95], v[128:131], v[44:47]\n"
    "ds_read_b64_tr_b16 v[92:93], v247 offset:112\n"
    "s_add_u32 %[st], %[hs], 12288\n"
    "buffer_load_dwordx4 v[116:119], v244, %[rs], %[st] offen sc1\n"
    "s_add_u32 %[st], %[hs], 28672\n"
    "buffer_load_dwordx4 v[120:123], v244, %[rs], %[st] offen sc1\n"
    "v_mfma_f32_16x16x32_bf16 v[48:51], v[96:99], v[124:127], v[48:51]\n"
    "ds_read_b64_tr_b16 v[94:95], v247 offset:1904\n"
    "v_mfma_f32_16x16x32_bf16 v[52:55], v[96:99], v[128:131], v[52:55]\n"
    "ds_read_b64_tr_b16 v[96:97], v247 offset:32\n"
    "s_waitcnt lgkmcnt(9)\n"
    "v_mfma_f32_16x16x32_bf16 v[56:59], v[100:103], v[124:127], v[56:59]\n"
    "ds_read_b64_tr_b16 v[98:99], v247 offset:1824\n"
    "v_mfma_f32_16x16x32_bf16 v[60:63], v[100:103], v[128:131], v[60:63]\n"
    "ds_read_b64_tr_b16 v[100:101], v247 offset:144\n"
    "v_mfma_f32_16x16x32_bf16 v[64:67], v[104:107], v[124:127], v[64:67]\n"
    "ds_read_b64_tr_b16 v[102:103], v247 offset:1936\n"
    "v_mfma_f32_16x16x32_bf16 v[68:71], v[104:107], v[128:131], v[68:71]\n"
    "ds_read_b64_tr_b16 v[104:105], v247 offset:64\n"
    "s_waitcnt lgkmcnt(9)\n"
    "v_mfma_f32_16x16x32_bf16 v[72:75], v[108:111], v[124:127], v[72:75]\n"
    "ds_read_b64_tr_b16 v[106:107], v247 offset:1856\n"
    "v_mfma_f32_16x16x32_bf16 v[76:79], v[108:111], v[128:131], v[76:79]\n"
    "ds_read_b64_tr_b16 v[108:109], v247 offset:176\n"
    "v_mfma_f32_16x16x32_bf16 v[80:83], v[112:115], v[124:127], v[80:83]\n"
    "ds_read_b64_tr_b16 v[110:111], v247 offset:1968\n"
    "v_mfma_f32_16x16x32_bf16 v[84:87], v[112:115], v[128:131], v[84:87]\n"
    "ds_read_b64_tr_b16 v[112:113], v248 offset:0\n"
    "s_waitcnt vmcnt(8)\n"
    "s_waitcnt lgkmcnt(7)\n"
    "v_mfma_f32_16x16x32_bf16 v[32:35], v[88:91], v[132:135], v[32:35]\n"
    "ds_read_b64_tr_b16 v[114:115], v248 offset:1792\n"
    "v_mfma_f32_16x16x32_bf16 v[36:39], v[88:91], v[136:139], v[36:39]\n"
    "ds_read_b64_tr_b16 v[88:89], v247 offset:7168\n"
    "v_mfma_f32_16x16x32_bf16 v[40:43], v[92:95], v[132:135], v[40:43]\n"
    "ds_read_b64_tr_b16 v[90:91], v247 offset:8960\n"
    "v_mfma_f32_16x16x32_bf16 v[44:47], v[92:95], v[136:139], v[44:47]\n"
    "ds_read_b64_tr_b16 v[92:93], v247 offset:7280\n"
    "s_add_u32 %[st], %[hs], 13312\n"
    "buffer_load_dwordx4 v[124:127], v244, %[rs], %[st] offen sc1\n"
    "s_add_u32 %[st], %[hs], 29696\n"
    "buffer_load_dwordx4 v[128:131], v244, %[rs], %[st] offen sc1\n"
    "v_mfma_f32_16x16x32_bf16 v[48:51], v[96:99], v[132:135], v[48:51]\n"
    "ds_read_b64_tr_b16 v[94:95], v247 offset:9072\n"
    "v_mfma_f32_16x16x32_bf16 v[52:55], v[96:99], v[136:139], v[52:55]\n"
    "ds_read_b64_tr_b16 v[96:97], v247 offset:7200\n"
    "s_waitcnt lgkmcnt(9)\n"
    "v_mfma_f32_16x16x32_bf16 v[56:59], v[100:103], v[132:135], v[56:59]\n"
    "ds_read_b64_tr_b16 v[98:99], v247 offset:8992\n"
    "v_mfma_f32_16x16x32_bf16 v[60:63], v[100:103], v[136:139], v[60:63]\n"
    "ds_read_b64_tr_b16 v[100:101], v247 offset:7312\n"
    "s_add_u32 %[st], %[ds], 4096\n"
    "buffer_load_dwordx4 v[172:175], v244, %[rs], %[st] offen sc1\n"
    "v_mfma_f32_16x16x32_bf16 v[64:67], v[104:107], v[132:135], v[64:67]\n"
    "ds_read_b64_tr_b16 v[102:103], v247 offset:9104\n"
    "v_mfma_f32_16x16x32_bf16 v[68:71], v[104:107], v[136:139], v[68:71]\n"
    "ds_read_b64_tr_b16 v[104:105], v247 offset:7232\n"
    "s_waitcnt lgkmcnt(9)\n"
    "v_mfma_f32_16x16x32_bf16 v[72:75], v[108:111], v[132:135], v[72:75]\n"
    "ds_read_b64_tr_b16 v[106:107], v247 offset:9024\n"
    "v_mfma_f32_16x16x32_bf16 v[76:79], v[108:111], v[136:139], v[76:79]\n"
    "ds_read_b64_tr_b16 v[108:109], v247 offset:7344\n"
    "s_add_u32 %[st], %[ds], 5120\n"
    "buffer_load_dwordx4 v[176:179], v244, %[rs], %[st] offen sc1\n"
    "v_mfma_f32_16x16x32_bf16 v[80:83], v[112:115], v[132:135], v[80:83]\n"
    "ds_read_b64_tr_b16 v[110:111], v247 offset:9136\n"
    "v_mfma_f32_16x16x32_bf16 v[84:87], v[112:115], v[136:139], v[84:87]\n"
    "ds_read_b64_tr_b16 v[112:113], v248 offset:7168\n"
    "s_waitcnt vmcnt(10)\n"
    "s_waitcnt lgkmcnt(7)\n"
    "v_mfma_f32_16x16x32_bf16 v[32:35], v[88:91], v[140:143], v[32:35]\n"
    "ds_read_b64_tr_b16 v[114:115], v248 offset:8960\n"
    "v_mfma_f32_16x16x32_bf16 v[36:39], v[88:91], v[144:147], v[36:39]\n"
    "ds_read_b64_tr_b16 v[88:89], v247 offset:14336\n"
    "v_mfma_f32_16x16x32_bf16 v[40:43], v[92:95], v[140:143], v[40:43]\n"
    "ds_read_b64_tr_b16 v[90:91], v247 offset:16128\n"
    "v_mfma_f32_16x16x32_bf16 v[44:47], v[92:95], v[144:147], v[44:47]\n"
    "ds_read_b64_tr_b16 v[92:93], v247 offset:14448\n"
    "s_add_u32 %[st], %[hs], 14336\n"
    "buffer_load_dwordx4 v[132:135], v244, %[rs], %[st] offen sc1\n"
    "s_add_u32 %[st], %[hs], 30720\n"
    "buffer_load_dwordx4 v[136:139], v244, %[rs], %[st] offen sc1\n"
    "v_mfma_f32_16x16x32_bf16 v[48:51], v[96:99], v[140:143], v[48:51]\n"
    "ds_read_b64_tr_b16 v[94:95], v247 offset:16240\n"
    "v_mfma_f32_16x16x32_bf16 v[52:55], v[96:99], v[144:147], v[52:55]\n"
    "ds_read_b64_tr_b16 v[96:97], v247 offset:14368\n"
    "s_waitcnt lgkmcnt(9)\n"
    "v_mfma_f32_16x16x32_bf16 v[56:59], v[100:103], v[140:143], v[56:59]\n"
    "ds_read_b64_tr_b16 v[98:99], v247 offset:16160\n"
    "v_mfma_f32_16x16x32_bf16 v[60:63], v[100:103], v[144:147], v[60:63]\n"
    "ds_read_b64_tr_b16 v[100:101], v247 offset:14480\n"
    "s_add_u32 %[st], %[ds], 6144\n"
    "buffer_load_dwordx4 v[180:183], v244, %[rs], %[st] offen sc1\n"
    "v_mfma_f32_16x16x32_bf16 v[64:67], v[104:107], v[140:143], v[64:67]\n"
    "ds_read_b64_tr_b16 v[102:103], v247 offset:16272\n"
    "v_mfma_f32_16x16x32_bf16 v[68:71], v[104:107], v[144:147], v[68:71]\n"
    "ds_read_b64_tr_b16 v[104:105], v247 offset:14400\n"
    "s_waitcnt lgkmcnt(9)\n"
    "v_mfma_f32_16x16x32_bf16 v[72:75], v[108:111], v[140:143], v[72:75]\n"
    "ds_read_b64_tr_b16 v[106:107], v247 offset:16192\n"
    "v_mfma_f32_16x16x32_bf16 v[76:79], v[108:111], v[144:147], v[76:79]\n"
    "ds_read_b64_tr_b16 v[108:109], v247 offset:14512\n"
    "s_add_u32 %[st], %[ds], 7168\n"
    "buffer_load_dwordx4 v[184:187], v244, %[rs], %[st] offen sc1\n"
    "v_mfma_f32_16x16x32_bf16 v[80:83], v[112:115], v[140:143], v[80:83]\n"
    "ds_read_b64_tr_b16 v[110:111], v247 offset:16304\n"
    "v_mfma_f32_16x16x32_bf16 v[84:87], v[112:115], v[144:147], v[84:87]\n"
    "ds_read_b64_tr_b16 v[112:113], v248 offset:14336\n"
    "s_waitcnt vmcnt(12)\n"
    "s_waitcnt lgkmcnt(7)\n"
    "v_mfma_f32_16x16x32_bf16 v[32:35], v[88:91], v[148:151], v[32:35]\n"
    "ds_read_b64_tr_b16 v[114:115], v248 offset:16128\n"
    "v_mfma_f32_16x16x32_bf16 v[36:39], v[88:91], v[152:155], v[36:39]\n"
    "ds_read_b64_tr_b16 v[88:89], v247 offset:21504\n"
    "v_mfma_f32_16x16x32_bf16 v[40:43], v[92:95], v[148:151], v[40:43]\n"
    "ds_read_b64_tr_b16 v[90:91], v247 offset:23296\n"
    "v_mfma_f32_16x16x32_bf16 v[44:47], v[92:95], v[152:155], v[44:47]\n"
    "ds_read_b64_tr_b16 v[92:93], v247 offset:21616\n"
    "s_add_u32 %[st], %[hs], 15360\n"
    "buffer_load_dwordx4 v[140:143], v244, %[rs], %[st] offen sc1\n"
    "s_add_u32 %[st], %[hs], 31744\n"
    "buffer_load_dwordx4 v[144:147], v244, %[rs], %[st] offen sc1\n"
    "v_mfma_f32_16x16x32_bf16 v[48:51], v[96:99], v[148:151], v[48:51]\n"
    "ds_read_b64_tr_b16 v[94:95], v247 offset:23408\n"
    "v_mfma_f32_16x16x32_bf16 v[52:55], v[96:99], v[152:155], v[52:55]\n"
    "ds_read_b64_tr_b16 v[96:97], v247 offset:21536\n"
    "s_waitcnt lgkmcnt(9)\n"
    "v_mfma_f32_16x16x32_bf16 v[56:59], v[100:103], v[148:151], v[56:59]\n"
    "ds_read_b64_tr_b16 v[98:99], v247 offset:23328\n"
    "v_mfma_f32_16x16x32_bf16 v[60:63], v[100:103], v[152:155], v[60:63]\n"
    "ds_read_b64_tr_b16 v[100:101], v247 offset:21648\n"
    "s_add_u32 %[st], %[ds], 8192\n"
    "buffer_load_dwordx4 v[0:3], v244, %[rs], %[st] offen sc1\n"
    "v_mfma_f32_16x16x32_bf16 v[64:67], v[104:107], v[148:151], v[64:67]\n"
    "ds_read_b64_tr_b16 v[102:103], v247 offset:23440\n"
    "v_mfma_f32_16x16x32_bf16 v[68:71], v[104:107], v[152:155], v[68:71]\n"
    "ds_read_b64_tr_b16 v[104:105], v247 offset:21568\n"
    "s_waitcnt lgkmcnt(9)\n"
    "v_mfma_f32_16x16x32_bf16 v[72:75], v[108:111], v[148:151], v[72:75]\n"
    "ds_read_b64_tr_b16 v[106:107], v247 offset:23360\n"
    "v_mfma_f32_16x16x32_bf16 v[76:79], v[108:111], v[152:155], v[76:79]\n"
    "ds_read_b64_tr_b16 v[108:109], v247 offset:21680\n"
    "s_add_u32 %[st], %[ds], 9216\n"
    "buffer_load_dwordx4 v[4:7], v244, %[rs], %[st] offen sc1\n"
    "v_mfma_f32_16x16x32_bf16 v[80:83], v[112:115], v[148:151], v[80:83]\n"
    "ds_read_b64_tr_b16 v[110:111], v247 offset:23472\n"
    "v_mfma_f32_16x16x32_bf16 v[84:87], v[112:115], v[152:155], v[84:87]\n"
    "ds_read_b64_tr_b16 v[112:113], v248 offset:21504\n"
    "s_waitcnt vmcnt(14)\n"
    "s_waitcnt lgkmcnt(7)\n"
    "v_mfma_f32_16x16x32_bf16 v[32:35], v[88:91], v[236:239], v[32:35]\n"
    "ds_read_b64_tr_b16 v[114:115], v248 offset:23296\n"
    "v_mfma_f32_16x16x32_bf16 v[36:39], v[88:91], v[240:243], v[36:39]\n"
    "ds_read_b64_tr_b16 v[88:89], v247 offset:28672\n"
    "v_mfma_f32_16x16x32_bf16 v[40:43], v[92:95], v[236:239], v[40:43]\n"
    "ds_read_b64_tr_b16 v[90:91], v247 offset:30464\n"
    "v_mfma_f32_16x16x32_bf16 v[44:47], v[92:95], v[240:243], v[44:47]\n"
    "ds_read_b64_tr_b16 v[92:93], v247 offset:28784\n"
    "v_mfma_f32_16x16x32_bf16 v[48:51], v[96:99], v[236:239], v[48:51]\n"
    "ds_read_b64_tr_b16 v[94:95], v247 offset:30576\n"
    "v_mfma_f32_16x16x32_bf16 v[52:55], v[96:99], v[240:243], v[52:55]\n"
    "ds_read_b64_tr_b16 v[96:97], v247 offset:28704\n"
    "s_waitcnt lgkmcnt(9)\n"
    "v_mfma_f32_16x16x32_bf16 v[56:59], v[100:103], v[236:239], v[56:59]\n"
    "ds_read_b64_tr_b16 v[98:99], v247 offset:30496\n"
    "v_mfma_f32_16x16x32_bf16 v[60:63], v[100:103], v[240:243], v[60:63]\n"
    "ds_read_b64_tr_b16 v[100:101], v247 offset:28816\n"
    "s_add_u32 %[st], %[ds], 10240\n"
    "buffer_load_dwordx4 v[8:11], v244, %[rs], %[st] offen sc1\n"
    "v_mfma_f32_16x16x32_bf16 v[64:67], v[104:107], v[236:239], v[64:67]\n"
    "ds_read_b64_tr_b16 v[102:103], v247 offset:30608\n"
    "v_mfma_f32_16x16x32_bf16 v[68:71], v[104:107], v[240:243], v[68:71]\n"
    "ds_read_b64_tr_b16 v[104:105], v247 offset:28736\n"
    "s_waitcnt lgkmcnt(9)\n"
    "v_mfma_f32_16x16x32_bf16 v[72:75], v[108:111], v[236:239], v[72:75]\n"
    "ds_read_b64_tr_b16 v[106:107], v247 offset:30528\n"
    "v_mfma_f32_16x16x32_bf16 v[76:79], v[108:111], v[240:243], v[76:79]\n"
    "ds_read_b64_tr_b16 v[108:109], v247 offset:28848\n"
    "s_add_u32 %[st], %[ds], 11264\n"
    "buffer_load_dwordx4 v[12:15], v244, %[rs], %[st] offen sc1\n"
    "v_mfma_f32_16x16x32_bf16 v[80:83], v[112:115], v[236:239], v[80:83]\n"
    "ds_read_b64_tr_b16 v[110:111], v247 offset:30640\n"
    "v_mfma_f32_16x16x32_bf16 v[84:87], v[112:115], v[240:243], v[84:87]\n"
    "ds_read_b64_tr_b16 v[112:113], v248 offset:28672\n"
    "s_waitcnt vmcnt(14)\n"
    "s_waitcnt lgkmcnt(7)\n"
    "v_mfma_f32_16x16x32_bf16 v[32:35], v[88:91], v[116:119], v[32:35]\n"
    "ds_read_b64_tr_b16 v[114:115], v248 offset:30464\n"
    "v_mfma_f32_16x16x32_bf16 v[36:39], v[88:91], v[120:123], v[36:39]\n"
    "ds_read_b64_tr_b16 v[88:89], v247 offset:35840\n"
    "v_mfma_f32_16x16x32_bf16 v[40:43], v[92:95], v[116:119], v[40:43]\n"
    "ds_read_b64_tr_b16 v[90:91], v247 offset:37632\n"
    "v_mfma_f32_16x16x32_bf16 v[44:47], v[92:95], v[120:123], v[44:47]\n"
    "ds_read_b64_tr_b16 v[92:93], v247 offset:35952\n"
    "v_mfma_f32_16x16x32_bf16 v[48:51], v[96:99], v[116:119], v[48:51]\n"
    "ds_read_b64_tr_b16 v[94:95], v247 offset:37744\n"
    "v_mfma_f32_16x16x32_bf16 v[52:55], v[96:99], v[120:123], v[52:55]\n"
    "ds_read_b64_tr_b16 v[96:97], v247 offset:35872\n"
    "s_waitcnt lgkmcnt(9)\n"
    "v_mfma_f32_16x16x32_bf16 v[56:59], v[100:103], v[116:119], v[56:59]\n"
    "ds_read_b64_tr_b16 v[98:99], v247 offset:37664\n"
    "v_mfma_f32_16x16x32_bf16 v[60:63], v[100:103], v[120:123], v[60:63]\n"
    "ds_read_b64_tr_b16 v[100:101], v247 offset:35984\n"
    "s_add_u32 %[st], %[ds], 12288\n"
    "buffer_load_dwordx4 v[16:19], v244, %[rs], %[st] offen sc1\n"
    "v_mfma_f32_16x16x32_bf16 v[64:67], v[104:107], v[116:119], v[64:67]\n"
    "ds_read_b64_tr_b16 v[102:103], v247 offset:37776\n"
    "v_mfma_f32_16x16x32_bf16 v[68:71], v[104:107], v[120:123], v[68:71]\n"
    "ds_read_b64_tr_b16 v[104:105], v247 offset:35904\n"
    "s_waitcnt lgkmcnt(9)\n"
    "v_mfma_f32_16x16x32_bf16 v[72:75], v[108:111], v[116:119], v[72:75]\n"
    "ds_read_b64_tr_b16 v[106:107], v247 offset:37696\n"
    "v_mfma_f32_16x16x32_bf16 v[76:79], v[108:111], v[120:123], v[76:79]\n"
    "ds_read_b64_tr_b16 v[108:109], v247 offset:36016\n"
    "s_add_u32 %[st], %[ds], 13312\n"
    "buffer_load_dwordx4 v[20:23], v244, %[rs], %[st] offen sc1\n"
    "v_mfma_f32_16x16x32_bf16 v[80:83], v[112:115], v[116:119], v[80:83]\n"
    "ds_read_b64_tr_b16 v[110:111], v247 offset:37808\n"
    "v_mfma_f32_16x16x32_bf16 v[84:87], v[112:115], v[120:123], v[84:87]\n"
    "ds_read_b64_tr_b16 v[112:113], v248 offset:35840\n"
    "s_waitcnt vmcnt(14)\n"
    "s_waitcnt lgkmcnt(7)\n"
    "v_mfma_f32_16x16x32_bf16 v[32:35], v[88:91], v[124:127], v[32:35]\n"
    "ds_read_b64_tr_b16 v[114:115], v248 offset:37632\n"
    "v_mfma_f32_16x16x32_bf16 v[36:39], v[88:91], v[128:131], v[36:39]\n"
    "ds_read_b64_tr_b16 v[88:89], v247 offset:43008\n"
    "v_mfma_f32_16x16x32_bf16 v[40:43], v[92:95], v[124:127], v[40:43]\n"
    "ds_read_b64_tr_b16 v[90:91], v247 offset:44800\n"
    "v_mfma_f32_16x16x32_bf16 v[44:47], v[92:95], v[128:131], v[44:47]\n"
    "ds_read_b64_tr_b16 v[92:93], v247 offset:43120\n"
    "v_mfma_f32_16x16x32_bf16 v[48:51], v[96:99], v[124:127], v[48:51]\n"
    "ds_read_b64_tr_b16 v[94:95], v247 offset:44912\n"
    "v_mfma_f32_16x16x32_bf16 v[52:55], v[96:99], v[128:131], v[52:55]\n"
    "ds_read_b64_tr_b16 v[96:97], v247 offset:43040\n"
    "s_waitcnt lgkmcnt(9)\n"
    "v_mfma_f32_16x16x32_bf16 v[56:59], v[100:103], v[124:127], v[56:59]\n"
    "ds_read_b64_tr_b16 v[98:99], v247 offset:44832\n"
    "v_mfma_f32_16x16x32_bf16 v[60:63], v[100:103], v[128:131], v[60:63]\n"
    "ds_read_b64_tr_b16 v[100:101], v247 offset:43152\n"
    "s_add_u32 %[st], %[ds], 14336\n"
    "buffer_load_dwordx4 v[24:27], v244, %[rs], %[st] offen sc1\n"
    "v_mfma_f32_16x16x32_bf16 v[64:67], v[104:107], v[124:127], v[64:67]\n"
    "ds_read_b64_tr_b16 v[102:103], v247 offset:44944\n"
    "v_mfma_f32_16x16x32_bf16 v[68:71], v[104:107], v[128:131], v[68:71]\n"
    "ds_read_b64_tr_b16 v[104:105], v247 offset:43072\n"
    "s_waitcnt lgkmcnt(9)\n"
    "v_mfma_f32_16x16x32_bf16 v[72:75], v[108:111], v[124:127], v[72:75]\n"
    "ds_read_b64_tr_b16 v[106:107], v247 offset:44864\n"
    "v_mfma_f32_16x16x32_bf16 v[76:79], v[108:111], v[128:131], v[76:79]\n"
    "ds_read_b64_tr_b16 v[108:109], v247 offset:43184\n"
    "s_add_u32 %[st], %[ds], 15360\n"
    "buffer_load_dwordx4 v[28:31], v244, %[rs], %[st] offen sc1\n"
    "v_mfma_f32_16x16x32_bf16 v[80:83], v[112:115], v[124:127], v[80:83]\n"
    "ds_read_b64_tr_b16 v[110:111], v247 offset:44976\n"
    "v_mfma_f32_16x16x32_bf16 v[84:87], v[112:115], v[128:131], v[84:87]\n"
    "ds_read_b64_tr_b16 v[112:113], v248 offset:43008\n"
    "s_waitcnt vmcnt(12)\n"
    "s_waitcnt lgkmcnt(7)\n"
    "v_mfma_f32_16x16x32_bf16 v[32:35], v[88:91], v[132:135], v[32:35]\n"
    "ds_read_b64_tr_b16 v[114:115], v248 offset:44800\n"
    "v_mfma_f32_16x16x32_bf16 v[36:39], v[88:91], v[136:139], v[36:39]\n"
    "ds_read_b64_tr_b16 v[88:89], v247 offset:50176\n"
    "v_mfma_f32_16x16x32_bf16 v[40:43], v[92:95], v[132:135], v[40:43]\n"
    "ds_read_b64_tr_b16 v[90:91], v247 offset:51968\n"
    "v_mfma_f32_16x16x32_bf16 v[44:47], v[92:95], v[136:139], v[44:47]\n"
    "ds_read_b64_tr_b16 v[92:93], v247 offset:50288\n"
    "v_mfma_f32_16x16x32_bf16 v[48:51], v[96:99], v[132:135], v[48:51]\n"
    "ds_read_b64_tr_b16 v[94:95], v247 offset:52080\n"
    "v_mfma_f32_16x16x32_bf16 v[52:55], v[96:99], v[136:139], v[52:55]\n"
    "ds_read_b64_tr_b16 v[96:97], v247 offset:50208\n"
    "s_waitcnt lgkmcnt(9)\n"
    "v_mfma_f32_16x16x32_bf16 v[56:59], v[100:103], v[132:135], v[56:59]\n"
    "ds_read_b64_tr_b16 v[98:99], v247 offset:52000\n"
    "v_mfma_f32_16x16x32_bf16 v[60:63], v[100:103], v[136:139], v[60:63]\n"
    "ds_read_b64_tr_b16 v[100:101], v247 offset:50320\n"
    "s_add_u32 %[st], %[ds], 24576\n"
    "buffer_load_dwordx4 v[220:223], v244, %[rs], %[st] offen sc1\n"
    "v_mfma_f32_16x16x32_bf16 v[64:67], v[104:107], v[132:135], v[64:67]\n"
    "ds_read_b64_tr_b16 v[102:103], v247 offset:52112\n"
    "v_mfma_f32_16x16x32_bf16 v[68:71], v[104:107], v[136:139], v[68:71]\n"
    "ds_read_b64_tr_b16 v[104:105], v247 offset:50240\n"
    "s_waitcnt lgkmcnt(9)\n"
    "v_mfma_f32_16x16x32_bf16 v[72:75], v[108:111], v[132:135], v[72:75]\n"
    "ds_read_b64_tr_b16 v[106:107], v247 offset:52032\n"
    "v_mfma_f32_16x16x32_bf16 v[76:79], v[108:111], v[136:139], v[76:79]\n"
    "ds_read_b64_tr_b16 v[108:109], v247 offset:50352\n"
    "s_add_u32 %[st], %[ds], 25600\n"
    "buffer_load_dwordx4 v[224:227], v244, %[rs], %[st] offen sc1\n"
    "v_mfma_f32_16x16x32_bf16 v[80:83], v[112:115], v[132:135], v[80:83]\n"
    "ds_read_b64_tr_b16 v[110:111], v247 offset:52144\n"
    "v_mfma_f32_16x16x32_bf16 v[84:87], v[112:115], v[136:139], v[84:87]\n"
    "ds_read_b64_tr_b16 v[112:113], v248 offset:50176\n"
    "s_waitcnt vmcnt(10)\n"
    "s_waitcnt lgkmcnt(11)\n"
    "v_mfma_f32_16x16x32_bf16 v[32:35], v[88:91], v[140:143], v[32:35]\n"
    "ds_read_b64_tr_b16 v[114:115], v248 offset:51968\n"
    "v_mfma_f32_16x16x32_bf16 v[36:39], v[88:91], v[144:147], v[36:39]\n"
    "s_waitcnt lgkmcnt(10)\n"
    "v_mfma_f32_16x16x32_bf16 v[40:43], v[92:95], v[140:143], v[40:43]\n"
    "v_mfma_f32_16x16x32_bf16 v[44:47], v[92:95], v[144:147], v[44:47]\n"
    "s_waitcnt lgkmcnt(8)\n"
    "v_mfma_f32_16x16x32_bf16 v[48:51], v[96:99], v[140:143], v[48:51]\n"
    "v_mfma_f32_16x16x32_bf16 v[52:55], v[96:99], v[144:147], v[52:55]\n"
    "s_waitcnt lgkmcnt(6)\n"
    "v_mfma_f32_16x16x32_bf16 v[56:59], v[100:103], v[140:143], v[56:59]\n"
    "v_mfma_f32_16x16x32_bf16 v[60:63], v[100:103], v[144:147], v[60:63]\n"
    "s_add_u32 %[st], %[ds], 26624\n"
    "buffer_load_dwordx4 v[228:231], v244, %[rs], %[st] offen sc1\n"
    "s_waitcnt lgkmcnt(4)\n"
    "v_mfma_f32_16x16x32_bf16 v[64:67], v[104:107], v[140:143], v[64:67]\n"
    "v_mfma_f32_16x16x32_bf16 v[68:71], v[104:107], v[144:147], v[68:71]\n"
    "s_waitcnt lgkmcnt(2)\n"
    "v_mfma_f32_16x16x32_bf16 v[72:75], v[108:111], v[140:143], v[72:75]\n"
    "v_mfma_f32_16x16x32_bf16 v[76:79], v[108:111], v[144:147], v[76:79]\n"
    "s_add_u32 %[st], %[ds], 27648\n"
    "buffer_load_dwordx4 v[232:235], v244, %[rs], %[st] offen sc1\n"
    "s_waitcnt lgkmcnt(0)\n"
    "v_mfma_f32_16x16x32_bf16 v[80:83], v[112:115], v[140:143], v[80:83]\n"
    "v_mfma_f32_16x16x32_bf16 v[84:87], v[112:115], v[144:147], v[84:87]\n"
    "s_nop 7\n"
                : "={v[32:47]}"(ac0), "={v[48:63]}"(ac1), "={v[64:79]}"(ac2), "={v[80:87]}"(ac3), "={v[220:235]}"(wdG), [st] "=&s"(st),
                  "={v[0:15]}"(wdC), "={v[16:31]}"(wdD), "+{v[156:171]}"(wdA), "={v[172:187]}"(wdB), "+{v[188:203]}"(wdE), "+{v[204:219]}"(wdF)
                : "{v244}"(l16b), "{v245}"(wl), "{v246}"(wm), [rs] "s"(wsr), [hs] "s"(hs), [ds] "s"(ds)
                :
                "v88", "v89", "v90", "v91", "v92", "v93", "v94", "v95", "v96", "v97", "v98", "v99", "v100", "v101", "v102", "v103", "v104", "v105", "v106", "v107", "v108", "v109", "v110", "v111", "v112", "v113", "v114", "v115", "v116", "v117", "v118", "v119", "v120", "v121", "v122", "v123", "v124", "v125", "v126", "v127", "v128", "v129", "v130", "v131", "v132", "v133", "v134", "v135", "v136", "v137", "v138", "v139", "v140", "v141", "v142", "v143", "v144", "v145", "v146", "v147", "v148", "v149", "v150", "v151", "v152", "v153", "v154", "v155", "v236", "v237", "v238", "v239", "v240", "v241", "v242", "v243", "v247", "v248"
                , "scc", "memory");
        }
#define S4(v, i) (f32x4){v[i], v[i + 1], v[i + 2], v[i + 3]}
        const f32x4 acc[7][2] = {{S4(ac0, 0), S4(ac0, 4)}, {S4(ac0, 8), S4(ac0, 12)}, {S4(ac1, 0), S4(ac1, 4)}, {S4(ac1, 8), S4(ac1, 12)}, {S4(ac2, 0), S4(ac2, 4)}, {S4(ac2, 8), S4(ac2, 12)}, {S4(ac3, 0), S4(ac3, 4)}};
        {
            const unsigned go = (1536 + nt * 64 + (wid & 3) * 16 + (lane >> 5) * 8) * 8;
            BLOAD1(gbv[0], wsr, go, WS_MOD, 0); BLOAD1(gbv[1], wsr, go, WS_MOD, 16); BLOAD1(gbv[2], wsr, go, WS_MOD, 32); BLOAD1(gbv[3], wsr, go, WS_MOD, 48);
        }
        const int l16 = lane & 15;
        const unsigned abase = WS_ACT + ((xg * 8 + wid) * 112 + (idx >> 1) * 7) * 1024;
        const int sg0 = 7 * (idx & 1);
        {
            f32x2 gt[8], up[8]; unsigned d[8];
#pragma unroll
            for (int i = 0; i < 8; ++i) { const f32x4 a = acc[(i & 2)][i >> 2], c = acc[(i & 2) + 1][i >> 2]; gt[i] = (i & 1) ? (f32x2){a[2], a[3]} : (f32x2){a[0], a[1]}; up[i] = (i & 1) ? (f32x2){c[2], c[3]} : (f32x2){c[0], c[1]}; }
            if (wid < 4) geglu_s<8>(gt, up, d); else geglu<8>(gt, up, d);
            const int sg = sg0 + g;
            const unsigned o = abase + (sg >> 1) * 1024 + ((sg & 1) * 32 + l16) * 16;
            st16_sc1(wsr, o, (u32x4){d[0], d[1], d[2], d[3]}); st16_sc1(wsr, o + 256, (u32x4){d[4], d[5], d[6], d[7]});
        }
        {
            f32x2 gt[6], up[6]; unsigned d[6];
#pragma unroll
            for (int i = 0; i < 4; ++i) { const f32x4 a = acc[4][i >> 1], c = acc[5][i >> 1]; gt[i] = (i & 1) ? (f32x2){a[2], a[3]} : (f32x2){a[0], a[1]}; up[i] = (i & 1) ? (f32x2){c[2], c[3]} : (f32x2){c[0], c[1]}; }
#pragma unroll
            for (int r = 0; r < 4; ++r) {
                const auto sw = __builtin_amdgcn_permlane32_swap(__float_as_uint(acc[6][0][r]), __float_as_uint(acc[6][1][r]), false, false);
                gt[4 + (r >> 1)][r & 1] = __uint_as_float(sw[0]); up[4 + (r >> 1)][r & 1] = __uint_as_float(sw[1]);
            }
            if (wid < 4) geglu_s<6>(gt, up, d); else geglu<6>(gt, up, d);
            const auto r0 = __builtin_amdgcn_permlane16_swap(d[0], d[2], false, false), r1 = __builtin_amdgcn_permlane16_swap(d[1], d[3], false, false);
            const int sg = sg0 + 4 + (g >> 1);
            st16_sc1(wsr, abase + (sg >> 1) * 1024 + ((sg & 1) * 32 + (g & 1) * 16 + l16) * 16, (u32x4){r0[0], r1[0], r0[1], r1[1]});
            const auto m0 = __builtin_amdgcn_permlane16_swap(d[4], d[4], false, false), m1 = __builtin_amdgcn_permlane16_swap(d[5], d[5], false, false);
            const int s6 = sg0 + 6;
            if (!(g & 1)) st16_sc1(wsr, abase + (s6 >> 1) * 1024 + ((s6 & 1) * 32 + (g >> 1) * 16 + l16) * 16, (u32x4){m0[0], m1[0], m0[1], m1[1]});
        }
        asm volatile("s_waitcnt vmcnt(0)" : "+v"(gbv[0]), "+v"(gbv[1]), "+v"(gbv[2]), "+v"(gbv[3]) :: "memory");
        PIN(wdB); PIN(wdC); PIN(wdD); PIN(wdG);
#define Q4(v, i) (u32x4){v[i], v[i + 1], v[i + 2], v[i + 3]}
        wdf[0][0] = Q4(wdA, 0); wdf[0][1] = Q4(wdA, 4); wdf[1][0] = Q4(wdA, 8); wdf[1][1] = Q4(wdA, 12); wdf[2][0] = Q4(wdB, 0); wdf[2][1] = Q4(wdB, 4); wdf[3][0] = Q4(wdB, 8); wdf[3][1] = Q4(wdB, 12);
        wdf[4][0] = Q4(wdC, 0); wdf[4][1] = Q4(wdC, 4); wdf[5][0] = Q4(wdC, 8); wdf[5][1] = Q4(wdC, 12); wdf[6][0] = Q4(wdD, 0); wdf[6][1] = Q4(wdD, 4); wdf[7][0] = Q4(wdD, 8); wdf[7][1] = Q4(wdD, 12);
        wdf[8][0] = Q4(wdE, 0); wdf[8][1] = Q4(wdE, 4); wdf[9][0] = Q4(wdE, 8); wdf[9][1] = Q4(wdE, 12); wdf[10][0] = Q4(wdF, 0); wdf[10][1] = Q4(wdF, 4); wdf[11][0] = Q4(wdF, 8); wdf[11][1] = Q4(wdF, 12);
        wdf[12][0] = Q4(wdG, 0); wdf[12][1] = Q4(wdG, 4); wdf[13][0] = Q4(wdG, 8); wdf[13][1] = Q4(wdG, 12);
        if (lane < 8) st8_sc1(wsr, WS_F4 + lane * 32768 + (((xg * 4 + (wid >> 1)) * 32 + idx) * 2 + (wid & 1)) * 16, tag);
    }
    {
        int lane = lane_id();
        const int tk = lane & 31, hf = lane >> 5;
        const int ocol = nt * 64 + (wid & 3) * 16 + hf * 8;
#pragma unroll
        for (int i = 0; i < 4; ++i) settle16<true>(gbv[i], wsr, WS_MOD + (1536 + ocol + 2 * i) * 8, tag);
        __builtin_amdgcn_s_barrier();
        unsigned l16b = lane * 16, as = WS_ACT + (m64 * 2 * 112 + wid * 14) * 1024;
        unsigned orow = (m64 * 64 + (wid >> 2) * 32 + tk) * DM + ocol;
        asm volatile("" : "+v"(l16b), "+v"(orow));
        asm volatile("" : "+s"(as));
        poll8(wsr, WS_F4 + nt * 32768 + ((m64 * 32 + 4 * wid) * 2 + (lane & 7)) * 16, tag);
        u32x4 af[28], x2v;
#define F2_LA(i) BLOAD1(af[i], wsr, l16b, as + ((i) < 14 ? (i) : 98 + (i)) * 1024, 0)
#pragma unroll
        for (int i = 0; i < 10; ++i) F2_LA(i);
        f32x16 a0, a1, a2, a3;
        const f32x16 z16 = {};
        float gb[8];
#pragma unroll
        for (int i = 0; i < 4; ++i) { gb[2 * i] = bflo(gbv[i][0]); gb[2 * i + 1] = bflo(gbv[i][2]); PIN(gb[2 * i]); PIN(gb[2 * i + 1]); }
        LAS char* pw = (LAS char*)smem + wid * 2048 + lane * 16;
        LAS char* pw2 = pw + 65536 - 1024; PIN(pw2);
#define F2_WP(pc) ((pc) < 4 ? pw + (pc) * 16384 : pw2 + 1024 + ((pc) - 4) * 16384)
#define F2_WR(pc, a) do { *(LAS f32x4*)(F2_WP(pc)) = (f32x4){a[((pc) & 1) * 8], a[((pc) & 1) * 8 + 1], a[((pc) & 1) * 8 + 2], a[((pc) & 1) * 8 + 3]}; \
                          *(LAS f32x4*)(F2_WP(pc) + 1024) = (f32x4){a[((pc) & 1) * 8 + 4], a[((pc) & 1) * 8 + 5], a[((pc) & 1) * 8 + 6], a[((pc) & 1) * 8 + 7]}; SBAR(); } while (0)
#define F2_STEP(i, N, A, B) do { asm volatile("s_waitcnt vmcnt(" #N ")" : "+v"(af[i]) :: "memory"); \
        A = mfma32(wdf[(i) % 14][0], af[i], (i) % 14 ? A : z16); B = mfma32(wdf[(i) % 14][1], af[i], (i) % 14 ? B : z16); SBAR(); \
        if ((i) + 10 < 28) F2_LA(((i) + 10) % 28); if ((i) == 18) BLOAD1(x2v, wsr, orow * 2, WS_X2, 0); } while (0)
        F2_STEP(0, 9, a0, a1); F2_STEP(1, 9, a0, a1); F2_STEP(2, 9, a0, a1); F2_STEP(3, 9, a0, a1); F2_STEP(4, 9, a0, a1); F2_STEP(5, 9, a0, a1); F2_STEP(6, 9, a0, a1);
        F2_STEP(7, 9, a0, a1); F2_STEP(8, 9, a0, a1); F2_STEP(9, 9, a0, a1); F2_STEP(10, 9, a0, a1); F2_STEP(11, 9, a0, a1); F2_STEP(12, 9, a0, a1); F2_STEP(13, 9, a0, a1);
        F2_STEP(14, 9, a2, a3); F2_STEP(15, 9, a2, a3); F2_WR(0, a0); F2_STEP(16, 9, a2, a3); F2_STEP(17, 9, a2, a3); F2_WR(1, a0); F2_STEP(18, 9, a2, a3); F2_STEP(19, 9, a2, a3);
        F2_WR(2, a1); F2_STEP(20, 8, a2, a3); F2_STEP(21, 7, a2, a3); F2_WR(3, a1); F2_STEP(22, 6, a2, a3); F2_STEP(23, 5, a2, a3); F2_STEP(24, 4, a2, a3);
        F2_STEP(25, 3, a2, a3); F2_STEP(26, 2, a2, a3); F2_STEP(27, 1, a2, a3);
        F2_WR(4, a2); F2_WR(5, a2); F2_WR(6, a3); F2_WR(7, a3);
        LDS_BAR();
        f32x4 p0[8], p1[8];
#pragma unroll
        for (int sl = 0; sl < 8; ++sl) { p0[sl] = *(const f32x4*)(smem + wid * 16384 + sl * 2048 + lane * 16); p1[sl] = *(const f32x4*)(smem + wid * 16384 + sl * 2048 + 1024 + lane * 16); }
        SBAR();
#pragma unroll
        for (int st = 1; st < 8; st *= 2)
#pragma unroll
            for (int sl = 0; sl < 8; sl += 2 * st) { p0[sl] += p0[sl + st]; p1[sl] += p1[sl + st]; PIN(p0[sl]); PIN(p1[sl]); }
        const f32x4 r0 = p0[0], r1 = p1[0];
        float y[8];
#pragma unroll
        for (int e = 0; e < 4; ++e) {
            const auto sw = __builtin_amdgcn_permlane32_swap(__float_as_uint(r0[e]), __float_as_uint(r1[e]), false, false);
            y[e] = __uint_as_float(sw[0]); y[4 + e] = __uint_as_float(sw[1]);
        }
        asm volatile("s_waitcnt vmcnt(0)" : "+v"(x2v) :: "memory");
        u32x4 o;
#pragma unroll
        for (int i = 0; i < 4; ++i) o[i] = cvtpk(__builtin_fmaf(gb[2 * i], y[2 * i], bflo(x2v[i])), __builtin_fmaf(gb[2 * i + 1], y[2 * i + 1], bfhi(x2v[i])));
        *(__attribute__((address_space(1))) u32x4*)(out + orow) = o;
    }
}

extern "C" void kernel_launch(void* const* d_in, const int* in_sizes, int n_in, void* d_out, int out_size, void* d_ws, size_t ws_size, hipStream_t stream) {
    (void)hipFuncSetAttribute((const void*)fused_block, hipFuncAttributeMaxDynamicSharedMemorySize, LDS_BYTES);
    fused_block<<<256, 512, LDS_BYTES, stream>>>((const uint16_t*)d_in[0], (const uint16_t*)d_in[1], (const uint16_t*)d_in[2], (const uint16_t*)d_in[3], (const uint16_t*)d_in[4],
                                                 (const uint16_t*)d_in[6], (const uint16_t*)d_in[7], (const uint16_t*)d_in[8], (const uint16_t*)d_in[9], (const uint16_t*)d_in[10],
                                                 (char*)d_ws, (uint16_t*)d_out);
}
```
